```python
import torch

def _softmax(x, axis=-1):
    return torch.softmax(x, dim=axis)
def _gelu(x):
    return torch.nn.functional.gelu(x, approximate="tanh")
def _silu(x):
    return torch.nn.functional.silu(x)
def _sigmoid(x):
    return torch.sigmoid(x)
def _layernorm(x, axis=-1, eps=1e-5):
    m = x.mean(dim=axis, keepdim=True)
    v = x.var(dim=axis, keepdim=True, unbiased=False)
    return (x - m) / torch.sqrt(v + eps)
def _scatter_add(tgt, idx, src, axis=0):
    return tgt.clone().index_add_(axis, idx, src)
def _segment_sum(src, seg, n):
    out = torch.zeros((n,) + src.shape[1:], dtype=src.dtype)
    return out.index_add_(0, seg, src)
def _pad(x, pw):
    flat = tuple(v for pair in reversed(pw) for v in pair)
    return torch.nn.functional.pad(x, flat)

kernel_name = 'interleave_layers__sliding_block_cached__swiglu_ffn__decode'

def setup_inputs():

    _w = (lambda a: (lambda t: t.to(torch.bfloat16) if t.is_floating_point() else t)(torch.as_tensor(a)))
    def _A_setup():
        B, Sq, D = 1, 1, 448
        Hq, Hkv, Sk, Dh = 16, 8, 65536, 256
        assert Hq % Hkv == 0, (Hq, Hkv)
        return {
            "x": _w((lambda *s: torch.randn(*s, dtype=torch.bfloat16) * 0.05)(B, Sq, D)),
            "k_cache": _w((lambda *s: torch.randn(*s, dtype=torch.bfloat16) * 1.6)(B, Hkv, Sk, Dh)),
            "v_cache": _w((lambda *s: torch.randn(*s, dtype=torch.bfloat16) * 1.6)(B, Hkv, Sk, Dh)),
            "wq": _w((lambda *s: torch.randn(*s, dtype=torch.bfloat16) * 1.6)(D, Hq * Dh) * (D ** -0.5)),
            "wo": _w((lambda *s: torch.randn(*s, dtype=torch.bfloat16) * 1.6)(Hq * Dh, D) * ((Hq * Dh) ** -0.5)),
        }
    _A_in = _A_setup()

    def _B_setup():
        B, S, D, F = 1, 1, 448, 1792
        return {
            "x": _w((lambda *s: torch.randn(*s, dtype=torch.bfloat16) * 0.05)(B, S, D)),
            "w_gate": _w((lambda *s: torch.randn(*s, dtype=torch.bfloat16) * 0.05)(D, F)),
            "w_up": _w((lambda *s: torch.randn(*s, dtype=torch.bfloat16) * 0.05)(D, F)),
            "w_down": _w((lambda *s: torch.randn(*s, dtype=torch.bfloat16) * 0.05)(F, D)),
        }
    _B_in = _B_setup()

    return {
        "A_x": _A_in["x"],
        "A_k_cache": _A_in["k_cache"],
        "A_v_cache": _A_in["v_cache"],
        "A_wq": _A_in["wq"],
        "A_wo": _A_in["wo"],
        "B_x": _B_in["x"],
        "B_w_gate": _B_in["w_gate"],
        "B_w_up": _B_in["w_up"],
        "B_w_down": _B_in["w_down"],
    }

def reference(A_x, A_k_cache, A_v_cache, A_wq, A_wo, B_x, B_w_gate, B_w_up, B_w_down):
    def _A_ref(x, k_cache, v_cache, wq, wo):
        B, Sq, D = x.shape
        Hq, Dh = 16, 256
        Hkv, Sk = k_cache.shape[1], k_cache.shape[2]
        W = min(4096, max(256, Sk // 4))
        rep = Hq // Hkv
        q = (lambda x, *d: x.permute(*d))((x @ wq).reshape(B, Sq, Hq, Dh), 0, 2, 1, 3)
        k = (lambda x, n, axis: torch.repeat_interleave(x, n, dim=axis))(k_cache[:, :, Sk - W:, :], rep, axis=1)
        v = (lambda x, n, axis: torch.repeat_interleave(x, n, dim=axis))(v_cache[:, :, Sk - W:, :], rep, axis=1)
        scores = torch.einsum("bhqd,bhkd->bhqk", q, k) * (Dh ** -0.5)
        p = _softmax(scores, axis=-1)
        o = torch.einsum("bhqk,bhkd->bhqd", p, v)
        return (lambda x, *d: x.permute(*d))(o, 0, 2, 1, 3).reshape(B, Sq, Hq * Dh) @ wo

    def _B_ref(x, w_gate, w_up, w_down):
        g = _silu(x @ w_gate)
        u = x @ w_up
        return (g * u) @ w_down

    x = A_x
    x = x + _A_ref(_layernorm(x, axis=-1), A_k_cache, A_v_cache, A_wq, A_wo)
    x = x + _B_ref(_layernorm(x, axis=-1), B_w_gate, B_w_up, B_w_down)
    x = x + _A_ref(_layernorm(x, axis=-1), A_k_cache, A_v_cache, A_wq, A_wo)
    x = x + _B_ref(_layernorm(x, axis=-1), B_w_gate, B_w_up, B_w_down)
    return x
```

```cpp
#include <hip/hip_runtime.h>
#include <hip/hip_bf16.h>
#include <stdint.h>

typedef unsigned short u16;
typedef unsigned v4u __attribute__((ext_vector_type(4)));

constexpr int D = 448, HQ = 16, HKV = 8, SK = 65536, DH = 256, W = 4096, F = 1792;

__device__ __forceinline__ float bf2f(u16 b) { return __uint_as_float(((unsigned)b) << 16); }
__device__ __forceinline__ float rbf(float f) { return (float)(__bf16)f; }
__device__ __forceinline__ u16 f2bf(float f) { __bf16 b = (__bf16)f; return __builtin_bit_cast(u16, b); }

__device__ float block_sum(float v, float* red) {
  for (int o = 32; o > 0; o >>= 1) v += __shfl_xor(v, o);
  int w = threadIdx.x >> 6, nw = blockDim.x >> 6;
  __syncthreads();
  if ((threadIdx.x & 63) == 0) red[w] = v;
  __syncthreads();
  float s = 0; for (int i = 0; i < nw; ++i) s += red[i];
  return s;
}
__device__ void layernorm(const float* x, float* xn, float* red) {
  float s = 0;
  for (int i = threadIdx.x; i < D; i += blockDim.x) s += x[i];
  float mean = block_sum(s, red) / D;
  float s2 = 0;
  for (int i = threadIdx.x; i < D; i += blockDim.x) { float d = x[i] - mean; s2 += d * d; }
  float var = block_sum(s2, red) / D;
  float mb = rbf(mean), vb = rbf(var);
  float den = rbf(sqrtf(rbf(vb + 1e-5f)));
  for (int i = threadIdx.x; i < D; i += blockDim.x) xn[i] = rbf(rbf(x[i] - mb) / den);
  __syncthreads();
}

__global__ void k_init(const u16* x, float* xf) { int i = threadIdx.x; if (i < D) xf[i] = bf2f(x[i]); }

__global__ void k_q(const float* xf, const u16* wq, float* q) {
  __shared__ float xn[D]; __shared__ float red[16];
  layernorm(xf, xn, red);
  int n = blockIdx.x * 256 + threadIdx.x;
  float a = 0;
  for (int k = 0; k < D; ++k) a += xn[k] * bf2f(wq[(size_t)k * (HQ * DH) + n]);
  q[n] = rbf(a);
}
__global__ void k_attn(const float* q, const u16* kc, const u16* vc, float* pm, float* pl, float* po) {
  __shared__ float qs[2][DH]; __shared__ float sc[2][128]; __shared__ float red[16];
  int g = blockIdx.x >> 5, s = blockIdx.x & 31, t = threadIdx.x;
  qs[0][t] = q[(2 * g) * DH + t]; qs[1][t] = q[(2 * g + 1) * DH + t];
  __syncthreads();
  const u16* kb = kc + ((size_t)g * SK + (SK - W) + s * 128) * DH;
  const u16* vb = vc + ((size_t)g * SK + (SK - W) + s * 128) * DH;
  { int h = t >> 7, key = t & 127; float a = 0;
    for (int d = 0; d < DH; ++d) a += qs[h][d] * bf2f(kb[key * DH + d]);
    sc[h][key] = rbf(a) * 0.0625f; }
  __syncthreads();
  for (int h = 0; h < 2; ++h) {
    float m = -INFINITY; for (int k = 0; k < 128; ++k) m = fmaxf(m, sc[h][k]);
    float l = 0, o = 0;
    for (int k = 0; k < 128; ++k) { float p = __expf(sc[h][k] - m); l += p; o += p * bf2f(vb[k * DH + t]); }
    int idx = (2 * g + h) * 32 + s;
    po[idx * DH + t] = o; if (t == 0) { pm[idx] = m; pl[idx] = l; }
  }
}
__global__ void k_comb(const float* pm, const float* pl, const float* po, float* o) {
  int h = blockIdx.x, t = threadIdx.x;
  float M = -INFINITY; for (int s = 0; s < 32; ++s) M = fmaxf(M, pm[h * 32 + s]);
  float L = 0, a = 0;
  for (int s = 0; s < 32; ++s) { float e = __expf(pm[h * 32 + s] - M); L += e * pl[h * 32 + s]; a += e * po[(h * 32 + s) * DH + t]; }
  o[h * DH + t] = rbf(a / L);
}
__global__ void k_proj_res(const float* in, int K, const u16* w, const float* xin, float* xout, u16* outb) {
  __shared__ float red[16];
  int n = blockIdx.x; float a = 0;
  for (int k = threadIdx.x; k < K; k += 256) a += in[k] * bf2f(w[(size_t)k * D + n]);
  a = block_sum(a, red);
  if (threadIdx.x == 0) { float r = rbf(xin[n] + rbf(a)); xout[n] = r; if (outb) outb[n] = f2bf(r); }
}
__global__ void k_gu(const float* xf, const u16* wg, const u16* wu, float* h) {
  __shared__ float xn[D]; __shared__ float red[16];
  layernorm(xf, xn, red);
  int n = blockIdx.x * 256 + threadIdx.x;
  float a = 0, b = 0;
  for (int k = 0; k < D; ++k) { a += xn[k] * bf2f(wg[(size_t)k * F + n]); b += xn[k] * bf2f(wu[(size_t)k * F + n]); }
  a = rbf(a); b = rbf(b);
  float g = rbf(a / (1.f + __expf(-a)));
  h[n] = rbf(g * b);
}

extern "C" void kernel_launch(void* const* d_in, const int* in_sizes, int n_in, void* d_out, int out_size, void* d_ws, size_t ws_size, hipStream_t stream) {
  const u16* x = (const u16*)d_in[0]; const u16* kc = (const u16*)d_in[1]; const u16* vc = (const u16*)d_in[2];
  const u16* wq = (const u16*)d_in[3]; const u16* wo = (const u16*)d_in[4];
  const u16* wg = (const u16*)d_in[6]; const u16* wu = (const u16*)d_in[7]; const u16* wd = (const u16*)d_in[8];
  float* ws = (float*)d_ws;
  float* xa = ws; float* xb = ws + 512; float* q = ws + 1024; float* o = q + 4096; float* h = o + 4096;
  float* pm = h + 2048; float* pl = pm + 512; float* po = pl + 512;
  k_init<<<1, 512, 0, stream>>>(x, xa);
  for (int l = 0; l < 2; ++l) {
    k_q<<<16, 256, 0, stream>>>(xa, wq, q);
    k_attn<<<256, 256, 0, stream>>>(q, kc, vc, pm, pl, po);
    k_comb<<<16, 256, 0, stream>>>(pm, pl, po, o);
    k_proj_res<<<448, 256, 0, stream>>>(o, 4096, wo, xa, xb, nullptr);
    k_gu<<<7, 256, 0, stream>>>(xb, wg, wu, h);
    k_proj_res<<<448, 256, 0, stream>>>(h, F, wd, xb, xa, l == 1 ? (u16*)d_out : nullptr);
  }
}
```

```cpp
#include <hip/hip_runtime.h>
#include <stdint.h>

typedef unsigned short u16;
typedef unsigned v4u __attribute__((ext_vector_type(4)));
typedef unsigned v2u __attribute__((ext_vector_type(2)));
typedef short v4s __attribute__((ext_vector_type(4)));
typedef float f32x4 __attribute__((ext_vector_type(4)));
typedef float f32x2 __attribute__((ext_vector_type(2)));
typedef __bf16 bf16x8 __attribute__((ext_vector_type(8)));
typedef __bf16 bf16x2 __attribute__((ext_vector_type(2)));
#define LAS __attribute__((address_space(3)))
#define GAS __attribute__((address_space(1)))

constexpr int D = 448, SK = 65536, DH = 256, WIN = 4096, FF = 1792, NQ = 4096;

constexpr int WS_EPOCH = 0;
constexpr int WS_Q = 4096;
constexpr int WS_ST = WS_Q + 32768;
constexpr int WS_X1P = WS_ST + 8192;
constexpr int WS_H = WS_X1P + 2 * 28672;
constexpr int WS_X2 = WS_H + 2 * 4096;
constexpr int WS_PO = 131072;
constexpr int WS_END = WS_PO + 2 * 524288;
constexpr int WS_STAMP = 4 << 20;
constexpr int L_WQ = 0, L_GU = 14336, L_WO = 28672, L_WD = 45056, L_SLAB = 53248, L_ML = L_SLAB + 16384, L_Q = L_ML + 128,
              L_O = L_Q + 1024, L_X = L_O + 512, L_X1 = L_X + 1024, L_END = 96 * 1024;

#define DQ 0
#define D2 0
#define D3 8
#define D3X 5
#define D4 8
#define D5 8
#define KVNT " nt"
constexpr unsigned SPIN_CAP = 1u << 17;
constexpr unsigned SENT = 0xFFFFFFFFu;

__device__ __forceinline__ f32x4 mfma(v4u a, v4u b, f32x4 c) {
  return __builtin_amdgcn_mfma_f32_16x16x32_bf16(__builtin_bit_cast(bf16x8, a), __builtin_bit_cast(bf16x8, b), c, 0, 0, 0);
}
__device__ __forceinline__ float dot2(unsigned a, unsigned b, float c) {
  return __builtin_amdgcn_fdot2_f32_bf16(__builtin_bit_cast(bf16x2, a), __builtin_bit_cast(bf16x2, b), c, false);
}
__device__ __forceinline__ unsigned pkbf(float lo, float hi) {
  f32x2 f = {lo, hi};
  return __builtin_bit_cast(unsigned, __builtin_convertvector(f, bf16x2));
}
__device__ __forceinline__ float rbf(float f) { return __uint_as_float(pkbf(f, f) & 0xFFFF0000u); }
__device__ __forceinline__ float fu(unsigned u) { return __uint_as_float(u); }
__device__ __forceinline__ unsigned uf(float f) { return __float_as_uint(f); }
template <int CTRL> __device__ __forceinline__ float dpp(float x) {
  return __builtin_bit_cast(float, __builtin_amdgcn_mov_dpp(__builtin_bit_cast(int, x), CTRL, 0xf, 0xf, true));
}
__device__ __forceinline__ float swap32_add(float a, float b) {
  auto s = __builtin_amdgcn_permlane32_swap(uf(a), uf(b), false, false);
  return fu(s[0]) + fu(s[1]);
}
__device__ __forceinline__ float swap16_add(float a, float b) {
  auto s = __builtin_amdgcn_permlane16_swap(uf(a), uf(b), false, false);
  return fu(s[0]) + fu(s[1]);
}
__device__ __forceinline__ float swap32_max(float a) {
  auto s = __builtin_amdgcn_permlane32_swap(uf(a), uf(a), false, false);
  return fmaxf(fu(s[0]), fu(s[1]));
}
__device__ __forceinline__ float swap16_max(float a) {
  auto s = __builtin_amdgcn_permlane16_swap(uf(a), uf(a), false, false);
  return fmaxf(fu(s[0]), fu(s[1]));
}
__device__ __forceinline__ float row_sum16(float v) {
  v += dpp<0x128>(v); v += dpp<0x124>(v); v += dpp<0x122>(v); v += dpp<0x121>(v);
  return v;
}
__device__ __forceinline__ unsigned max3u(unsigned a, unsigned b, unsigned c) { return max(max(a, b), c); }
__device__ __forceinline__ unsigned max4u(v4u v, unsigned acc) { return max3u(max3u(acc, v.x, v.y), v.z, v.w); }
__device__ __forceinline__ unsigned tagacc(v4u v, unsigned t, unsigned acc) {
  acc |= v.x ^ t; acc |= v.y ^ t; acc |= v.z ^ t; acc |= v.w ^ t; return acc;
}
__device__ __forceinline__ void glds16(const void* gsrc, unsigned lds_dst) {
  unsigned keep;
  asm volatile("s_mov_b32 %0, m0\n\ts_mov_b32 m0, %2\n\ts_nop 0\n\tglobal_load_lds_dwordx4 %1, off\n\ts_mov_b32 m0, %0"
               : "=&s"(keep) : "v"(gsrc), "s"(lds_dst) : "memory");
}
__device__ __forceinline__ void glds4(const void* gsrc, unsigned lds_dst) {
  unsigned keep;
  asm volatile("s_mov_b32 %0, m0\n\ts_mov_b32 m0, %2\n\ts_nop 0\n\tglobal_load_lds_dword %1, off\n\ts_mov_b32 m0, %0"
               : "=&s"(keep) : "v"(gsrc), "s"(lds_dst) : "memory");
}
#define KVLD(dst, voff, sbase, imm) \
  asm volatile("global_load_dwordx4 %0, %1, %2 offset:" #imm KVNT : "=v"(dst) : "v"(voff), "s"(sbase) : "memory")
#define BAR() do { asm volatile("s_waitcnt lgkmcnt(0)" ::: "memory"); __builtin_amdgcn_s_barrier(); asm volatile("" ::: "memory"); } while (0)
#define VMWAIT() asm volatile("s_waitcnt vmcnt(0)" ::: "memory")
#define LD128(off) __builtin_amdgcn_raw_buffer_load_b128(rs, (off), 0, 16)
#define ST128(v, off) __builtin_amdgcn_raw_buffer_store_b128((v), rs, (off), 0, 16)
#define ST64(v, off) __builtin_amdgcn_raw_buffer_store_b64((v), rs, (off), 0, 16)
#define ST32(v, off) __builtin_amdgcn_raw_buffer_store_b32((v), rs, (off), 0, 16)
#define SLEEP(n) do { if ((n) > 0) __builtin_amdgcn_s_sleep(n); } while (0)

#define STAMP(k) do {} while (0)
#define CNT(k, v) do {} while (0)

__global__ void __launch_bounds__(576)
fused_decode(const u16* __restrict__ xin, const u16* __restrict__ kc, const u16* __restrict__ vc, const u16* __restrict__ wq,
             const u16* __restrict__ wo, const u16* __restrict__ wg, const u16* __restrict__ wu, const u16* __restrict__ wd,
             unsigned* __restrict__ out, unsigned char* ws) {
  __shared__ __attribute__((aligned(16))) unsigned char smem[L_END];
  LAS unsigned char* L = (LAS unsigned char*)smem;
  const unsigned lbase = (unsigned)(uintptr_t)L;
  const int tid = threadIdx.x, lane = tid & 63, wave = __builtin_amdgcn_readfirstlane(tid >> 6);
  const int c = lane & 15, G = lane >> 4;
  const int b = blockIdx.x, g = b & 7, r = b >> 3, hh = r >> 4, j = r & 15;
  const bool active = j < 14;
  const int jj = active ? j : j - 14;
  const int pid = g * 28 + hh * 14 + jj;
  const int h = 2 * g + hh;
  const __amdgpu_buffer_rsrc_t rs = __builtin_amdgcn_make_buffer_rsrc(ws, 0, WS_END, 0x00020000);
  STAMP(0);
  const int troff = (8 * G + (c >> 2)) * 32 + (c & 3) * 8;
  auto load_frag = [&](int base) -> v4u {
    v4s lo = __builtin_amdgcn_ds_read_tr16_b64_v4i16((LAS v4s*)(L + base + troff));
    v4s hi = __builtin_amdgcn_ds_read_tr16_b64_v4i16((LAS v4s*)(L + base + troff + 128));
    v2u a = __builtin_bit_cast(v2u, lo), bb = __builtin_bit_cast(v2u, hi);
    return (v4u){a.x, a.y, bb.x, bb.y};
  };

  if (wave < 8) {
    v4u K0, K1, K2, K3, K4, K5, K6, K7, V0, V1, V2, V3, V4, V5, V6, V7;
    {
      const size_t kvbase = ((size_t)g * SK + (SK - WIN) + r * 128 + wave * 16) * DH;
      const u16* kb = kc + kvbase;
      const u16* vb = vc + kvbase;
      const unsigned ko = c * 512 + G * 16, vo = G * 2048 + c * 16;
      KVLD(K0, ko, kb, 0); KVLD(K1, ko, kb, 64); KVLD(K2, ko, kb, 128); KVLD(K3, ko, kb, 192);
      KVLD(K4, ko, kb, 256); KVLD(K5, ko, kb, 320); KVLD(K6, ko, kb, 384); KVLD(K7, ko, kb, 448);
      KVLD(V0, vo, vb, 0); KVLD(V1, vo, vb, 256); KVLD(V2, vo, vb, 512); KVLD(V3, vo, vb, 768);
      KVLD(V4, vo, vb, 1024); KVLD(V5, vo, vb, 1280); KVLD(V6, vo, vb, 1536); KVLD(V7, vo, vb, 1792);
    }
    if (wave >= 6) {
      const int t = wave - 6;
      const u16* src = wo + (size_t)(h * 256 + (lane >> 1)) * D + jj * 32 + t * 16 + (lane & 1) * 8;
#pragma unroll
      for (int s = 0; s < 8; ++s) glds16(src + (size_t)s * 32 * D, lbase + L_WO + t * 8192 + s * 1024);
    } else if (wave >= 4) {
      const int t = wave - 4;
      const u16* src = ((lane & 1) ? wu : wg) + (size_t)(t * 224 + (lane >> 1)) * FF + pid * 8;
#pragma unroll
      for (int s = 0; s < 7; ++s) glds16(src + (size_t)s * 32 * FF, lbase + L_GU + t * 7168 + s * 1024);
    } else {
      const u16* sd = wd + (size_t)(wave * 448 + lane) * D + pid * 2;
#pragma unroll
      for (int s = 0; s < 7; ++s) glds4(sd + (size_t)s * 64 * D, lbase + L_WD + wave * 1792 + s * 256);
    }
    unsigned epoch;
    asm volatile("s_load_dword %0, %1, 0x0\n\ts_waitcnt lgkmcnt(0)" : "=s"(epoch) : "s"(ws) : "memory");
    epoch += 1u; if ((epoch & 0xFFFu) == 0xFFFu) epoch += 1u;
    const unsigned tag12 = epoch & 0xFFFu;
    STAMP(1);
    unsigned pm[4];
#pragma unroll
    for (int q = 0; q < 4; ++q) pm[q] = (((c & 7) >> 1) == q) ? ((c & 1) ? 0xFFFF0000u : 0x0000FFFFu) : 0u;

#pragma unroll 1
    for (int layer = 0; layer < 2; ++layer) {
      BAR();
      {
        const int qo = L_Q + (c >> 3) * 512 + G * 16;
        const v4u q0 = *(LAS v4u*)(L + qo), q1 = *(LAS v4u*)(L + qo + 64), q2 = *(LAS v4u*)(L + qo + 128), q3 = *(LAS v4u*)(L + qo + 192),
                  q4 = *(LAS v4u*)(L + qo + 256), q5 = *(LAS v4u*)(L + qo + 320), q6 = *(LAS v4u*)(L + qo + 384), q7 = *(LAS v4u*)(L + qo + 448);
        asm volatile("s_waitcnt vmcnt(15)" : "+v"(K0), "+v"(K1), "+v"(K2), "+v"(K3), "+v"(K4), "+v"(K5), "+v"(K6), "+v"(K7));
        f32x4 s = {0, 0, 0, 0};
        s = mfma(K0, q0, s); s = mfma(K1, q1, s); s = mfma(K2, q2, s); s = mfma(K3, q3, s);
        s = mfma(K4, q4, s); s = mfma(K5, q5, s); s = mfma(K6, q6, s); s = mfma(K7, q7, s);
        STAMP(4 + layer * 16);
        const float SC = 0.0625f * 1.4426950408889634f;
        const unsigned pk0 = pkbf(s[0], s[1]), pk1 = pkbf(s[2], s[3]);
        const float t0 = fu(pk0 << 16) * SC, t1 = fu(pk0 & 0xFFFF0000u) * SC, t2 = fu(pk1 << 16) * SC, t3 = fu(pk1 & 0xFFFF0000u) * SC;
        float m = fmaxf(fmaxf(t0, t1), fmaxf(t2, t3));
        m = swap16_max(m); m = swap32_max(m);
        const float p0 = __builtin_amdgcn_exp2f(t0 - m), p1 = __builtin_amdgcn_exp2f(t1 - m), p2 = __builtin_amdgcn_exp2f(t2 - m), p3 = __builtin_amdgcn_exp2f(t3 - m);
        float l = (p0 + p1) + (p2 + p3);
        l = swap16_add(l, l); l = swap32_add(l, l);
        f32x4 o0 = {0, 0, 0, 0}, o1 = {0, 0, 0, 0};
        unsigned pb; v4u pf;
        pb = pkbf(p0, p0); pf = (v4u){pb & pm[0], pb & pm[1], pb & pm[2], pb & pm[3]};
        asm volatile("s_waitcnt vmcnt(13)" : "+v"(V0), "+v"(V1));
        o0 = mfma(V0, pf, o0); o1 = mfma(V1, pf, o1);
        pb = pkbf(p1, p1); pf = (v4u){pb & pm[0], pb & pm[1], pb & pm[2], pb & pm[3]};
        asm volatile("s_waitcnt vmcnt(11)" : "+v"(V2), "+v"(V3));
        o0 = mfma(V2, pf, o0); o1 = mfma(V3, pf, o1);
        pb = pkbf(p2, p2); pf = (v4u){pb & pm[0], pb & pm[1], pb & pm[2], pb & pm[3]};
        asm volatile("s_waitcnt vmcnt(9)" : "+v"(V4), "+v"(V5));
        o0 = mfma(V4, pf, o0); o1 = mfma(V5, pf, o1);
        pb = pkbf(p3, p3); pf = (v4u){pb & pm[0], pb & pm[1], pb & pm[2], pb & pm[3]};
        asm volatile("s_waitcnt vmcnt(7)" : "+v"(V6), "+v"(V7));
        o0 = mfma(V6, pf, o0); o1 = mfma(V7, pf, o1);
        const int so = L_SLAB + wave * 2048 + (c >> 3) * 1024 + (G * 8 + (c & 7)) * 16;
        *(LAS f32x4*)(L + so) = o0;
        *(LAS f32x4*)(L + so + 512) = o1;
        if ((lane & 0x37) == 0) {
          *(LAS float*)(L + L_ML + (c >> 3) * 32 + wave * 4) = m;
          *(LAS float*)(L + L_ML + 64 + (c >> 3) * 32 + wave * 4) = l;
        }
        STAMP(5 + layer * 16);
      }
      BAR();
      {
        const int hd = wave >> 2;
        const f32x4 ma = *(LAS f32x4*)(L + L_ML + hd * 32), mb = *(LAS f32x4*)(L + L_ML + hd * 32 + 16);
        const f32x4 la = *(LAS f32x4*)(L + L_ML + 64 + hd * 32), lb = *(LAS f32x4*)(L + L_ML + 64 + hd * 32 + 16);
        float ov[8];
#pragma unroll
        for (int w = 0; w < 8; ++w) ov[w] = *(LAS float*)(L + L_SLAB + w * 2048 + tid * 4);
        const float M = fmaxf(fmaxf(fmaxf(ma[0], ma[1]), fmaxf(ma[2], ma[3])), fmaxf(fmaxf(mb[0], mb[1]), fmaxf(mb[2], mb[3])));
        float o = 0.f, l = 0.f;
#pragma unroll
        for (int w = 0; w < 4; ++w) {
          const float e = __builtin_amdgcn_exp2f(ma[w] - M), f = __builtin_amdgcn_exp2f(mb[w] - M);
          o += e * ov[w]; o += f * ov[4 + w]; l += e * la[w]; l += f * lb[w];
        }
        ST32(min(uf(o), 0xFFFFFFFEu), WS_PO + layer * 524288 + (g * 32 + r) * 2048 + tid * 4);
        if ((tid & 255) == 0) {
          const v2u ml = {min(uf(M), 0xFFFFFFFEu), min(uf(l), 0xFFFFFFFEu)};
          ST64(ml, WS_ST + layer * 4096 + g * 512 + hd * 256 + (r & 7) * 32 + (r >> 3) * 8);
        }
      }
      STAMP(6 + layer * 16);
      if (layer == 1 && !active) break;
      v4u W[8];
      {
        SLEEP(D2);
        const int a = lane >> 3, c8 = lane & 7;
        const int pb = WS_PO + layer * 524288 + (g * 32 + a) * 2048 + hh * 1024 + wave * 128 + c8 * 16;
        const int sb = WS_ST + layer * 4096 + g * 512 + hh * 256 + a * 32;
        v4u p0, p1, p2, p3, st0, st1;
        unsigned sp = 0;
        for (;;) {
          st0 = LD128(sb); st1 = LD128(sb + 16);
          p0 = LD128(pb); p1 = LD128(pb + 16384); p2 = LD128(pb + 32768); p3 = LD128(pb + 49152);
          const unsigned mx = max4u(p3, max4u(p2, max4u(p1, max4u(p0, max4u(st1, max4u(st0, 0u))))));
          if (__all(mx != SENT) || ++sp > SPIN_CAP) break;
          __builtin_amdgcn_s_sleep(1);
        }
        CNT(41 + layer * 8, sp);
        STAMP(7 + layer * 16);
        asm volatile("" ::: "memory");
        if (wave >= 6) {
#pragma unroll
          for (int s = 0; s < 8; ++s) W[s] = load_frag(L_WO + (wave - 6) * 8192 + s * 1024);
        }
        float M = fmaxf(fmaxf(fu(st0.x), fu(st0.z)), fmaxf(fu(st1.x), fu(st1.z)));
        M = fmaxf(M, dpp<0x128>(M)); M = swap16_max(M); M = swap32_max(M);
        const float e0 = __builtin_amdgcn_exp2f(fu(st0.x) - M), e1 = __builtin_amdgcn_exp2f(fu(st0.z) - M),
                    e2 = __builtin_amdgcn_exp2f(fu(st1.x) - M), e3 = __builtin_amdgcn_exp2f(fu(st1.z) - M);
        float Ls = e0 * fu(st0.y) + e1 * fu(st0.w) + e2 * fu(st1.y) + e3 * fu(st1.w);
        Ls += dpp<0x128>(Ls); Ls = swap16_add(Ls, Ls); Ls = swap32_add(Ls, Ls);
        float v[4];
#pragma unroll
        for (int i = 0; i < 4; ++i) v[i] = e0 * fu(p0[i]) + e1 * fu(p1[i]) + e2 * fu(p2[i]) + e3 * fu(p3[i]);
        float o = swap16_add(swap32_add(v[0], v[2]), swap32_add(v[1], v[3]));
        o += dpp<0x128>(o);
        o = o * __builtin_amdgcn_rcpf(Ls);
        if ((lane & 8) == 0) *(LAS u16*)(L + L_O + (wave * 32 + G * 8 + c8) * 2) = (u16)pkbf(o, o);
      }
      BAR();
      if (wave >= 6) {
        f32x4 acc = {0, 0, 0, 0};
#pragma unroll
        for (int s = 0; s < 8; ++s) acc = mfma(*(LAS v4u*)(L + L_O + s * 64 + G * 16), W[s], acc);
        if (lane < 16 && active) ST32((uf(acc[0]) & 0xFFFFF000u) | tag12, WS_X1P + layer * 28672 + h * 1792 + j * 128 + (wave - 6) * 64 + lane * 4);
        STAMP(8 + layer * 16);
      } else {
        SLEEP(D3X);
      }
      if (wave < 7) {
        SLEEP(D3);
        const int xb = WS_X1P + layer * 28672 + G * 1792 + wave * 256 + c * 16;
        const float xres = fu((unsigned)*(LAS u16*)(L + L_X + (wave * 64 + c * 4 + G) * 2) << 16);
        v4u p0, p1, p2, p3;
        unsigned sp = 0;
        for (;;) {
          p0 = LD128(xb); p1 = LD128(xb + 7168); p2 = LD128(xb + 14336); p3 = LD128(xb + 21504);
          const unsigned bad = tagacc(p3, tag12, tagacc(p2, tag12, tagacc(p1, tag12, tagacc(p0, tag12, 0u)))) & 0xFFFu;
          if (__all(bad == 0u) || ++sp > SPIN_CAP) break;
          __builtin_amdgcn_s_sleep(1);
        }
        CNT(42 + layer * 8, sp);
        STAMP(9 + layer * 16);
        float v[4];
#pragma unroll
        for (int i = 0; i < 4; ++i) v[i] = (fu(p0[i]) + fu(p1[i])) + (fu(p2[i]) + fu(p3[i]));
        const float a = swap16_add(swap32_add(v[0], v[2]), swap32_add(v[1], v[3]));
        const float x1 = xres + rbf(a);
        *(LAS u16*)(L + L_X1 + (wave * 64 + c * 4 + G) * 2) = (u16)pkbf(x1, x1);
      }
      BAR();
    }
  } else {
    const v4u sv = {SENT, SENT, SENT, SENT};
    v4u xc = {0, 0, 0, 0};
    if (lane < 56) xc = *(const GAS v4u*)(xin + lane * 8);
    {
      const u16* src = wq + (size_t)(lane >> 1) * NQ + g * 512 + r * 16 + (lane & 1) * 8;
#pragma unroll
      for (int s = 0; s < 14; ++s) glds16(src + (size_t)s * 32 * NQ, lbase + L_WQ + s * 1024);
    }
#pragma unroll
    for (int l = 0; l < 2; ++l) {
      const int o = WS_PO + l * 524288 + (g * 32 + r) * 2048 + lane * 16;
      ST128(sv, o); ST128(sv, o + 1024);
    }
    {
      const int k = lane >> 3;
      int o = WS_END;
      if (k == 0 && active) o = WS_X1P + h * 1792 + j * 128;
      if (k == 1 && active) o = WS_X1P + 28672 + h * 1792 + j * 128;
      if (k == 2 && !(r & 1)) o = WS_Q + 16384 + (g * 32 + r) * 64;
      if (k == 3 && r < 8) o = WS_ST + (r >> 2) * 4096 + g * 512 + (r & 3) * 128;
      if (k == 4 && b < 64) o = WS_H + b * 128;
      if (k == 5 && b < 7) o = WS_X2 + b * 128;
      ST128(sv, o + (lane & 7) * 16);
    }
    unsigned epoch;
    asm volatile("s_load_dword %0, %1, 0x0\n\ts_waitcnt lgkmcnt(0)" : "=s"(epoch) : "s"(ws) : "memory");
    epoch += 1u; if ((epoch & 0xFFFu) == 0xFFFu) epoch += 1u;
    const unsigned tag16 = epoch & 0xFFFFu;
    const v4u ones = {0x3F803F80u, 0x3F803F80u, 0x3F803F80u, 0x3F803F80u};
    auto ln_stats = [&](v4u x, float& mu, float& rstd) {
      float s1 = dot2(x.x, ones.x, 0.f), s2 = dot2(x.x, x.x, 0.f);
      s1 = dot2(x.y, ones.x, s1); s2 = dot2(x.y, x.y, s2);
      s1 = dot2(x.z, ones.x, s1); s2 = dot2(x.z, x.z, s2);
      s1 = dot2(x.w, ones.x, s1); s2 = dot2(x.w, x.w, s2);
      float t = swap32_add(s1, s2);
      t = swap16_add(t, t);
      t = row_sum16(t);
      const float S1 = fu(__builtin_amdgcn_readlane(uf(t), 0)), S2 = fu(__builtin_amdgcn_readlane(uf(t), 32));
      const float mean = S1 * (1.f / D);
      const float var = fmaxf(S2 * (1.f / D) - mean * mean, 0.f);
      mu = rbf(mean);
      const float den = rbf(__builtin_amdgcn_sqrtf(rbf(rbf(var) + 1e-5f)));
      rstd = __builtin_amdgcn_rcpf(den);
    };
    VMWAIT();
    STAMP(1);
    v4u W[14];
    float cs;
    {
      f32x4 a = {0, 0, 0, 0};
#pragma unroll
      for (int s = 0; s < 14; ++s) { W[s] = load_frag(L_WQ + s * 1024); a = mfma(ones, W[s], a); }
      cs = a[0];
    }
#pragma unroll 1
    for (int layer = 0; layer < 2; ++layer) {
      {
        if (lane < 56) *(LAS v4u*)(L + L_X + lane * 16) = xc;
        f32x4 acc = {0, 0, 0, 0};
#pragma unroll
        for (int s = 0; s < 14; ++s) acc = mfma(*(LAS v4u*)(L + L_X + s * 64 + G * 16), W[s], acc);
        float mu, rstd;
        ln_stats(xc, mu, rstd);
        const float qv = (acc[0] - mu * cs) * rstd;
        const int qb = WS_Q + layer * 16384 + g * 2048;
        if (lane < 16) ST32((pkbf(qv, qv) & 0xFFFF0000u) | tag16, qb + r * 64 + lane * 4);
        STAMP(2 + layer * 16);
        SLEEP(DQ);
        v4u a0, a1;
        unsigned sp = 0;
        for (;;) {
          a0 = LD128(qb + lane * 32); a1 = LD128(qb + lane * 32 + 16);
          const unsigned bad = tagacc(a1, tag16, tagacc(a0, tag16, 0u)) & 0xFFFFu;
          if (__all(bad == 0u) || ++sp > SPIN_CAP) break;
          __builtin_amdgcn_s_sleep(1);
        }
        CNT(40 + layer * 8, sp);
        v4u qf = {__builtin_amdgcn_perm(a0.y, a0.x, 0x07060302u), __builtin_amdgcn_perm(a0.w, a0.z, 0x07060302u),
                  __builtin_amdgcn_perm(a1.y, a1.x, 0x07060302u), __builtin_amdgcn_perm(a1.w, a1.z, 0x07060302u)};
        *(LAS v4u*)(L + L_Q + lane * 16) = qf;
        STAMP(3 + layer * 16);
      }
      BAR();
      BAR();
      if (layer == 1 && !active) break;
      BAR();
      v4u Wd[8];
      {
        f32x4 a = {0, 0, 0, 0};
#pragma unroll
        for (int s = 0; s < 14; ++s) { W[s] = load_frag(L_GU + s * 1024); a = mfma(ones, W[s], a); }
        cs = a[0];
#pragma unroll
        for (int u = 0; u < 4; ++u) {
          v4u w0 = *(LAS v4u*)(L + L_WD + (u * 56 + lane) * 32), w1 = *(LAS v4u*)(L + L_WD + (u * 56 + lane) * 32 + 16);
          if (lane >= 56) { w0 = (v4u){0, 0, 0, 0}; w1 = w0; }
          Wd[2 * u] = (v4u){__builtin_amdgcn_perm(w0.y, w0.x, 0x05040100u), __builtin_amdgcn_perm(w0.w, w0.z, 0x05040100u),
                            __builtin_amdgcn_perm(w1.y, w1.x, 0x05040100u), __builtin_amdgcn_perm(w1.w, w1.z, 0x05040100u)};
          Wd[2 * u + 1] = (v4u){__builtin_amdgcn_perm(w0.y, w0.x, 0x07060302u), __builtin_amdgcn_perm(w0.w, w0.z, 0x07060302u),
                                __builtin_amdgcn_perm(w1.y, w1.x, 0x07060302u), __builtin_amdgcn_perm(w1.w, w1.z, 0x07060302u)};
        }
        asm volatile("" : "+v"(cs), "+v"(Wd[0]), "+v"(Wd[1]), "+v"(Wd[2]), "+v"(Wd[3]), "+v"(Wd[4]), "+v"(Wd[5]), "+v"(Wd[6]), "+v"(Wd[7]));
      }
      BAR();
      f32x4 acc = {0, 0, 0, 0};
      v4u x1c = {0, 0, 0, 0};
      if (lane < 56) x1c = *(LAS v4u*)(L + L_X1 + lane * 16);
      const unsigned x1p = *(LAS unsigned*)(L + L_X1 + pid * 4);
#pragma unroll
      for (int s = 0; s < 14; ++s) acc = mfma(*(LAS v4u*)(L + L_X1 + s * 64 + G * 16), W[s], acc);
      float mu, rstd;
      ln_stats(x1c, mu, rstd);
      const float gu = rbf((acc[0] - mu * cs) * rstd);
      const float up = dpp<0x128>(gu);
      const float sg = rbf(gu * __builtin_amdgcn_rcpf(1.f + __builtin_amdgcn_exp2f(-1.4426950408889634f * gu)));
      const float hv = sg * up;
      const unsigned hp = pkbf(hv, dpp<0xB1>(hv));
      const int hb = WS_H + layer * 4096;
      if ((lane & 0x39) == 0 && active) ST32(min(hp, 0xFFFFFFFEu), hb + pid * 16 + lane * 2);
      STAMP(10 + layer * 16);
      if (layer == 0) {
        f32x4 a = {0, 0, 0, 0};
#pragma unroll
        for (int s = 0; s < 14; ++s) { W[s] = load_frag(L_WQ + s * 1024); a = mfma(ones, W[s], a); }
        cs = a[0];
        asm volatile("" : "+v"(cs));
        SLEEP(D4 - 5);
      } else {
        SLEEP(D4);
      }
      v4u h0, h1, h2, h3;
      const int ho = lane < 56 ? hb + lane * 16 : WS_END;
      unsigned sp = 0;
      for (;;) {
        h0 = LD128(ho); h1 = LD128(ho + 896); h2 = LD128(ho + 1792); h3 = LD128(ho + 2688);
        const unsigned mx = max4u(h3, max4u(h2, max4u(h1, max4u(h0, 0u))));
        if (__all(mx != SENT) || ++sp > SPIN_CAP) break;
        __builtin_amdgcn_s_sleep(1);
      }
      CNT(43 + layer * 8, sp);
      STAMP(11 + layer * 16);
      float a0 = 0.f, a1 = 0.f, b0 = 0.f, b1 = 0.f;
#pragma unroll
      for (int i = 0; i < 4; ++i) {
        a0 = dot2(h0[i], Wd[0][i], a0); a1 = dot2(h0[i], Wd[1][i], a1);
        b0 = dot2(h1[i], Wd[2][i], b0); b1 = dot2(h1[i], Wd[3][i], b1);
        a0 = dot2(h2[i], Wd[4][i], a0); a1 = dot2(h2[i], Wd[5][i], a1);
        b0 = dot2(h3[i], Wd[6][i], b0); b1 = dot2(h3[i], Wd[7][i], b1);
      }
      float t = swap32_add(a0 + b0, a1 + b1);
      t = swap16_add(t, t);
      t = row_sum16(t);
      const float y0 = fu(__builtin_amdgcn_readlane(uf(t), 0)), y1 = fu(__builtin_amdgcn_readlane(uf(t), 32));
      const unsigned x2p = pkbf(fu(x1p << 16) + rbf(y0), fu(x1p & 0xFFFF0000u) + rbf(y1));
      if (layer == 1) {
        if (lane == 0) {
          out[pid] = x2p;
          if (pid == 0) ST32(epoch, WS_EPOCH);
        }
        STAMP(12 + layer * 16);
      } else {
        if (lane == 0 && active) ST32(min(x2p, 0xFFFFFFFEu), WS_X2 + pid * 4);
        STAMP(12 + layer * 16);
        SLEEP(D5);
        const int xo = lane < 56 ? WS_X2 + lane * 16 : WS_END;
        sp = 0;
        for (;;) {
          xc = LD128(xo);
          if (__all(max4u(xc, 0u) != SENT) || ++sp > SPIN_CAP) break;
          __builtin_amdgcn_s_sleep(1);
        }
        CNT(44 + layer * 8, sp);
        STAMP(13 + layer * 16);
      }
    }
  }
}

extern "C" void kernel_launch(void* const* d_in, const int* in_sizes, int n_in, void* d_out, int out_size, void* d_ws,
                              size_t ws_size, hipStream_t stream) {
  fused_decode<<<256, 576, 0, stream>>>((const u16*)d_in[0], (const u16*)d_in[1], (const u16*)d_in[2], (const u16*)d_in[3],
                                        (const u16*)d_in[4], (const u16*)d_in[6], (const u16*)d_in[7], (const u16*)d_in[8],
                                        (unsigned*)d_out, (unsigned char*)d_ws);
}
```

```cpp
#include <hip/hip_runtime.h>
#include <stdint.h>

typedef unsigned short u16;
typedef unsigned long long u64;
typedef unsigned v4u __attribute__((ext_vector_type(4)));
typedef unsigned v2u __attribute__((ext_vector_type(2)));
typedef unsigned v16u __attribute__((ext_vector_type(16)));
typedef short v4s __attribute__((ext_vector_type(4)));
typedef float f32x4 __attribute__((ext_vector_type(4)));
typedef float f32x2 __attribute__((ext_vector_type(2)));
typedef __bf16 bf16x8 __attribute__((ext_vector_type(8)));
typedef __bf16 bf16x2 __attribute__((ext_vector_type(2)));
#define LAS __attribute__((address_space(3)))
#define GAS __attribute__((address_space(1)))

constexpr int D = 448, SK = 65536, WIN = 4096, FF = 1792;

constexpr int LAY = 2 << 20, BLK = 16384;
constexpr int WS_EPOCH = 0;
constexpr int WS_FLAG = 2048;
constexpr int WS_X2P = 1 * BLK;
constexpr int WS_H = 2 * BLK;
constexpr int WS_X1P = 3 * BLK;
constexpr int WS_ST = 7 * BLK;
constexpr int WS_QP = 11 * BLK;
constexpr int WS_CS = 19 * BLK;
constexpr int WS_PO = 27 * BLK;
constexpr int WS_END = 2 * LAY;
constexpr int WS_STAMP = 4 << 20;
constexpr int L_ML = 0, L_WT = 128, L_SC = 256, L_CS = 384, L_HM = 640, L_DP = 896, L_O = 1408, L_Q = 1920, QSTR = 576, L_X = L_Q + 1152,
              L_X1 = L_X + 1024, L_SLAB = L_X1 + 1024, L_WQ = L_SLAB + 16384, L_ROLE = L_WQ + 16384, L_END = L_ROLE + 57344;

#define CPAR 0
#define DQ 4
#define D2 4
#define DST 4
#define D3 30
#define D4 60
#define D5 80
#define TH 0
#define TQ0 8000
#define PSL 1
#define CODE_LINES 96
constexpr unsigned SPIN_CAP = 1u << 17;
constexpr unsigned SENT = 0xFFFFFFFFu;

__device__ __forceinline__ f32x4 mfma(v4u a, v4u b, f32x4 c) {
  return __builtin_amdgcn_mfma_f32_16x16x32_bf16(__builtin_bit_cast(bf16x8, a), __builtin_bit_cast(bf16x8, b), c, 0, 0, 0);
}
__device__ __forceinline__ float dot2(unsigned a, unsigned b, float c) {
  return __builtin_amdgcn_fdot2_f32_bf16(__builtin_bit_cast(bf16x2, a), __builtin_bit_cast(bf16x2, b), c, false);
}
__device__ __forceinline__ unsigned pkbf(float lo, float hi) {
  f32x2 f = {lo, hi};
  return __builtin_bit_cast(unsigned, __builtin_convertvector(f, bf16x2));
}
__device__ __forceinline__ float fu(unsigned u) { return __builtin_bit_cast(float, u); }
__device__ __forceinline__ unsigned uf(float f) { return __builtin_bit_cast(unsigned, f); }
__device__ __forceinline__ float rbf(float f) { return fu(pkbf(f, f) & 0xFFFF0000u); }
template <int CTRL> __device__ __forceinline__ float dpp(float x) {
  return __builtin_bit_cast(float, __builtin_amdgcn_mov_dpp(__builtin_bit_cast(int, x), CTRL, 0xf, 0xf, true));
}
__device__ __forceinline__ float fmax2(float a, float b) { return __builtin_fmaxf(a, b); }
__device__ __forceinline__ float swap32_add(float a, float b) {
  auto s = __builtin_amdgcn_permlane32_swap(uf(a), uf(b), false, false);
  return fu(s[0]) + fu(s[1]);
}
__device__ __forceinline__ float swap16_add(float a, float b) {
  auto s = __builtin_amdgcn_permlane16_swap(uf(a), uf(b), false, false);
  return fu(s[0]) + fu(s[1]);
}
__device__ __forceinline__ float swap32_max(float a) {
  auto s = __builtin_amdgcn_permlane32_swap(uf(a), uf(a), false, false);
  return fmax2(fu(s[0]), fu(s[1]));
}
__device__ __forceinline__ float swap16_max(float a) {
  auto s = __builtin_amdgcn_permlane16_swap(uf(a), uf(a), false, false);
  return fmax2(fu(s[0]), fu(s[1]));
}
__device__ __forceinline__ float row_sum16(float v) {
  v += dpp<0x128>(v); v += dpp<0x124>(v); v += dpp<0x122>(v); v += dpp<0x121>(v);
  return v;
}
__device__ __forceinline__ float row_max16(float v) {
  v = fmax2(v, dpp<0x128>(v)); v = fmax2(v, dpp<0x124>(v)); v = fmax2(v, dpp<0x122>(v)); v = fmax2(v, dpp<0x121>(v));
  return v;
}
__device__ __forceinline__ unsigned umax(unsigned a, unsigned b) { return a > b ? a : b; }
__device__ __forceinline__ unsigned umin(unsigned a, unsigned b) { return a < b ? a : b; }
__device__ __forceinline__ unsigned max4u(v4u v, unsigned acc) { return umax(umax(umax(umax(acc, v.x), v.y), v.z), v.w); }
__device__ __forceinline__ unsigned or4u(v4u v, unsigned acc) { return (acc | v.x | v.y) | v.z | v.w; }
__device__ __forceinline__ bool none_eq(unsigned v, unsigned x) { return __builtin_amdgcn_ballot_w64(v == x) == 0ull; }
__device__ __forceinline__ bool all_zero(unsigned v) { return __builtin_amdgcn_ballot_w64(v != 0u) == 0ull; }
__device__ __forceinline__ void glds16(unsigned voff, u64 sbase, unsigned lds_dst) {
  unsigned keep;
  asm volatile("s_mov_b32 %0, m0\n\ts_mov_b32 m0, %3\n\ts_nop 0\n\tglobal_load_lds_dwordx4 %1, %2 nt\n\ts_mov_b32 m0, %0"
               : "=&s"(keep) : "v"(voff), "s"(sbase), "s"(lds_dst) : "memory");
}
__device__ __forceinline__ void ln_stats(v4u x, float& mu, float& rstd) {
  const unsigned one2 = 0x3F803F80u;
  float s1 = dot2(x.x, one2, 0.f), s2 = dot2(x.x, x.x, 0.f);
  s1 = dot2(x.y, one2, s1); s2 = dot2(x.y, x.y, s2);
  s1 = dot2(x.z, one2, s1); s2 = dot2(x.z, x.z, s2);
  s1 = dot2(x.w, one2, s1); s2 = dot2(x.w, x.w, s2);
  float t = swap32_add(s1, s2);
  t = swap16_add(t, t);
  t = row_sum16(t);
  const float S1 = fu(__builtin_amdgcn_readlane(uf(t), 0)), S2 = fu(__builtin_amdgcn_readlane(uf(t), 32));
  const float mean = S1 * (1.f / D);
  const float var = fmax2(S2 * (1.f / D) - mean * mean, 0.f);
  mu = rbf(mean);
  const float den = rbf(__builtin_amdgcn_sqrtf(rbf(rbf(var) + 1e-5f)));
  rstd = __builtin_amdgcn_rcpf(den);
}
#define KVLD(dst, voff, sbase, imm) \
  asm volatile("global_load_dwordx4 %0, %1, %2 offset:" #imm " nt" : "=v"(dst) : "v"(voff), "s"(sbase) : "memory")
#define THR(i) do { if (TH && (i) >= TH) asm volatile("s_waitcnt vmcnt(%0)" :: "i"(TH > 0 ? TH - 1 : 0) : "memory"); } while (0)
#define BAR() do { asm volatile("s_waitcnt lgkmcnt(0)" ::: "memory"); __builtin_amdgcn_s_barrier(); asm volatile("" ::: "memory"); } while (0)
#define VMWAIT() asm volatile("s_waitcnt vmcnt(0)" ::: "memory")
#define FENCE() __builtin_amdgcn_sched_barrier(0)
#define PIN(x) asm volatile("" : "+v"(x))
#define LD128(off, so) __builtin_amdgcn_raw_buffer_load_b128(rs, (off), (so), 16)
#define LD32(off, so) __builtin_amdgcn_raw_buffer_load_b32(rs, (off), (so), 16)
#define ST128(v, off, so) __builtin_amdgcn_raw_buffer_store_b128((v), rs, (off), (so), 16)
#define ST64(v, off, so) __builtin_amdgcn_raw_buffer_store_b64((v), rs, (off), (so), 16)
#define ST32(v, off, so) __builtin_amdgcn_raw_buffer_store_b32((v), rs, (off), (so), 16)
#define SLEEP(n) do { if ((n) > 0) __builtin_amdgcn_s_sleep(n); } while (0)
#define POLL _Pragma("clang loop unroll(disable)") for (;;)
#define DONE_IF(cond) if (__builtin_expect((cond), 1) || ++sp > SPIN_CAP) break; __builtin_amdgcn_s_sleep(PSL)
#define LDS(T, off) (*(LAS T*)(L + (off)))
#define NOSENT(u) umin((u), 0xFFFFFFFEu)
#define TAGGED(f) ((uf(f) & 0xFFFFF000u) | tag12)

#define RSTAMP(k) do {} while (0)
#define STAMP(k) do {} while (0)
#define CNT(k, v) do {} while (0)
#define FLUSH_STAMPS() do {} while (0)

__global__ void __launch_bounds__(768)
fused_decode(const u16* xin_, const u16* kc_, const u16* vc_, const u16* wq_, const u16* wo_, const u16* wg_, const u16* wu_,
             const u16* wd_, unsigned* out_, unsigned char* ws_) {
  __shared__ __attribute__((aligned(16))) unsigned char smem[L_END];
  LAS unsigned char* L = (LAS unsigned char*)smem;
  const unsigned lbase = (unsigned)(uintptr_t)L;
  v16u ka; v4u kb4; u64 pc0;
  asm volatile("s_load_dwordx16 %0, %3, 0x0\n\ts_load_dwordx4 %1, %3, 0x40\n\ts_getpc_b64 %2"
               : "=&s"(ka), "=&s"(kb4), "=&s"(pc0) : "s"(__builtin_amdgcn_kernarg_segment_ptr()) : "memory");
  const int tid = threadIdx.x, lane_ = tid & 63, wv = __builtin_amdgcn_readfirstlane(tid >> 6);
  const int b = blockIdx.x, g = b & 7, r = b >> 3, cu = g * 32 + r;
  const int gq = g ^ 1, rb = r >> 3, cbq = r & 7, s0 = rb * 4 - (rb == 3);
  const bool svc = wv < 4;
  if (svc) __builtin_amdgcn_s_setprio(3); else if (wv < 8) __builtin_amdgcn_s_setprio(1);
  asm volatile("s_waitcnt lgkmcnt(0)" : "+s"(ka), "+s"(kb4));
  STAMP(0); RSTAMP(60);
#define PTR(v, i) ((u64)(v)[2 * (i)] | ((u64)(v)[2 * (i) + 1] << 32))
  if (wv < 2) {
    glds16(umin(lane_, 55) * 16u, PTR(ka, 0), lbase + L_X);
    u64 src = PTR(ka, 3) + (unsigned)(s0 * 32 * 8192 + (gq * 512 + cbq * 64 + wv * 32) * 2);
    const unsigned vo = (unsigned)(lane_ >> 2) * 8192u + (lane_ & 3) * 16u;
    const int n = rb >= 2 ? 6 : 8;
#pragma unroll 1
    for (int s = 0; s < n; ++s) { glds16(vo, src, lbase + L_WQ + wv * 8192 + s * 1024); src += 131072; }
  }
  const u64 wsp = PTR(kb4, 1);
  const __amdgpu_buffer_rsrc_t rs = __builtin_amdgcn_make_buffer_rsrc((void*)wsp, 0, WS_END, 0x00020000);
  const int role = r < 28 ? (int)((0x34221111u >> (g * 4)) & 7) : 0;
  const int q7 = (r * 37) >> 8, cbo = r - 7 * q7;
  const int go = (q7 & 2) ? 4 + g : (g ^ 1), hh = q7 & 1, h = 2 * go + hh;
  const int idg = (g - 4) * 28 + r, kb = r & 3, cbd = r >> 2;
  const int P0 = CPAR * 8192, P1 = 8192 - P0, PG = g < 4 ? P0 : P1;
  auto trfrag = [&](int addr, int step4) -> v4u {
    v4s lo = __builtin_amdgcn_ds_read_tr16_b64_v4i16((LAS v4s*)(L + addr));
    v4s hi = __builtin_amdgcn_ds_read_tr16_b64_v4i16((LAS v4s*)(L + addr + step4));
    v2u a = __builtin_bit_cast(v2u, lo), bb = __builtin_bit_cast(v2u, hi);
    return (v4u){a.x, a.y, bb.x, bb.y};
  };
  unsigned epoch, flaghi, tag12;
  auto load_epoch = [&]() {
    asm volatile("s_load_dword %0, %1, 0x0\n\ts_waitcnt lgkmcnt(0)" : "=s"(epoch) : "s"(wsp) : "memory");
    epoch += 1u;
    flaghi = epoch * 0x9E3779B1u + 0x7F4A7C15u;
    tag12 = epoch & 0xFFFu;
  };
  auto check_flags = [&]() {
    unsigned sp = 0;
    POLL {
      const v4u f0 = LD128(lane_ * 16, WS_FLAG), f1 = LD128(lane_ * 16, WS_FLAG + 1024);
      const unsigned bad = (f0.x ^ epoch) | (f0.y ^ flaghi) | (f0.z ^ epoch) | (f0.w ^ flaghi) | (f1.x ^ epoch) | (f1.y ^ flaghi) | (f1.z ^ epoch) | (f1.w ^ flaghi);
      DONE_IF(all_zero(bad));
    }
    CNT(45, sp);
  };

  if (svc) {
    const u64 tstart = __builtin_amdgcn_s_memtime();
    const unsigned one2 = 0x3F803F80u;
    const v4u ones = {one2, one2, one2, one2};
    int xoff, wqoff, qsto;
    {
      const int lane = lane_, c = lane & 15, G = lane >> 4;
      int ks = c + s0; ks -= ks >= 14 ? 14 : 0;
      xoff = c < 14 ? L_X + (4 * ks + G) * 16 : L_X + 896;
      wqoff = L_WQ + (wv & 1) * 8192 + (8 * G + (c >> 2)) * 64 + (c & 3) * 8;
      qsto = lane < 32 ? gq * BLK + rb * 2048 + cbq * 256 + wv * 128 + lane * 4 : WS_END;
      if (wv == 2) {
        const int ln = (r & 7) * (CODE_LINES / 8) + lane;
        if (lane < CODE_LINES / 8) { unsigned t = *(const GAS unsigned*)((pc0 & ~127ull) + ln * 128); asm volatile("" :: "v"(t)); }
      }
      load_epoch();
      if (wv == 3) {
        const v4u sv = {SENT, SENT, SENT, SENT};
        const int k = lane >> 3, l = k & 1, k2 = (k >> 1) & 1;
        int o2 = WS_END;
        if (k < 4) {
          if (role == 1) o2 = l * LAY + WS_X1P + (h >> 2) * BLK + (h & 3) * 2048 + cbo * 256 + k2 * 128 + (P1 ^ (l * 8192));
          else if (role == 2 && k < 2) o2 = l * LAY + WS_H + idg * 128 + (P1 ^ (l * 8192));
          else if (role == 4) o2 = WS_X2P + l * 8192 + kb * 2048 + cbd * 256 + k2 * 128;
        }
        ST128(sv, o2 + (lane & 7) * 16, 0);
        VMWAIT();
        const v2u f = {epoch, flaghi};
        ST64(f, lane == 0 ? b * 8 : WS_END, WS_FLAG);
      }
    }
    v4u Wq[8], xc = {0, 0, 0, 0};
    float csq = 0.f, mup = 0.f;
    f32x4 cst = {0, 0, 0, 0};
    auto load_wq = [&]() {
#pragma unroll
      for (int s = 0; s < 4; ++s) { Wq[s] = trfrag(wqoff + s * 2048, 256); Wq[4 + s] = trfrag(wqoff + s * 2048 + 32, 256); }
    };
    if (wv < 2) {
      const v4u zz = {0, 0, 0, 0};
      if (rb >= 2) { LDS(v4u, L_WQ + wv * 8192 + 6144 + lane_ * 16) = zz; LDS(v4u, L_WQ + wv * 8192 + 7168 + lane_ * 16) = zz; }
      VMWAIT();
      STAMP(1);
      load_wq();
      xc = LDS(v4u, xoff);
      if ((lane_ & 15) >= 14) xc = zz;
      f32x4 a0 = {0, 0, 0, 0}, a1 = {0, 0, 0, 0};
#pragma unroll
      for (int s = 0; s < 4; ++s) { a0 = mfma(ones, Wq[s], a0); a1 = mfma(ones, Wq[4 + s], a1); }
      csq = (lane_ & 16) ? a1[0] : a0[0];
      float rs_;
      ln_stats(xc, mup, rs_);
    }
#pragma unroll 1
    for (int layer = 0; layer < 2; ++layer) {
      const int lo = layer * LAY, lx = layer * 8192;
      if (wv < 2) {
        const f32x4 z = {0, 0, 0, 0};
        const f32x4 d0 = mfma(xc, Wq[0], z), e0 = mfma(xc, Wq[4], z), d1 = mfma(xc, Wq[1], z), e1 = mfma(xc, Wq[5], z);
        const f32x4 d2 = mfma(xc, Wq[2], z), e2 = mfma(xc, Wq[6], z), d3 = mfma(xc, Wq[3], z), e3 = mfma(xc, Wq[7], z);
        const float ta = ((d0[0] + d1[1]) + d2[2]) + d3[3], tb = ((e0[0] + e1[1]) + e2[2]) + e3[3];
        const float tq = fu(__builtin_amdgcn_permlane16_swap(uf(ta), uf(tb), false, false)[0]);
        ST32(TAGGED(tq - mup * csq), qsto, lo + WS_QP + (PG ^ lx));
        FENCE();
        STAMP(2 + layer * 16);
        float mu, rstd;
        ln_stats(xc, mu, rstd);
        LDS(float, L_SC) = rstd * (0.0625f * 1.4426950408889634f);
        f32x4 init = cst * -mu;
        PIN(init);
        if (layer == 0) {
          ST32(TAGGED(csq), qsto, WS_CS + PG);
          while (__builtin_amdgcn_s_memtime() - tstart < TQ0) __builtin_amdgcn_s_sleep(2);
        } else {
          SLEEP(DQ);
        }
        const int qa = wv * 1024 + lane_ * 16, qs = lo + WS_QP + g * BLK + (PG ^ lx);
        unsigned sp = 0;
        v2u qf;
        POLL {
          v4u a0 = LD128(qa, qs), a1 = LD128(qa + 2048, qs), a2 = LD128(qa, qs + 4096), a3 = LD128(qa + 2048, qs + 4096);
          a0 ^= tag12; a1 ^= tag12; a2 ^= tag12; a3 ^= tag12;
          const unsigned bad = or4u(a3, or4u(a2, or4u(a1, or4u(a0, 0u)))) & 0xFFFu;
          float v[4];
#pragma unroll
          for (int i = 0; i < 4; ++i) v[i] = (((init[i] + fu(a0[i])) + fu(a1[i])) + fu(a2[i])) + fu(a3[i]);
          qf = (v2u){pkbf(v[0], v[1]), pkbf(v[2], v[3])};
          DONE_IF(all_zero(bad));
        }
        LDS(v2u, L_Q + wv * QSTR + lane_ * 8) = qf;
        CNT(40 + layer * 8, sp);
        STAMP(3 + layer * 16);
      }
      BAR();
      int lane = lane_; PIN(lane);
      const int c = lane & 15, G = lane >> 4, trrow = 8 * G + (c >> 2), trp = c & 3;
      u64 sb = 0; unsigned vo = 0; int step = 0, n = 0, n0 = 0;
      const int nw = role == 1 ? 8 : (role == 2 ? 7 : 14);
      if (layer == 0) {
        if (role == 1) { sb = PTR(ka, 4); vo = ((h * 256 + wv * 64 + (lane >> 3)) * D + cbo * 64 + (lane & 7) * 8) * 2; step = 16 * D; n = 8; n0 = 8; }
        else if (role == 2) { sb = PTR(ka, 5); vo = ((wv * 112 + (lane >> 2)) * FF + idg * 32 + (lane & 3) * 8) * 2; step = 32 * FF; n = 14; n0 = 4; }
        else if (role == 4) { sb = PTR(ka, 7); vo = ((kb * 448 + wv * 112 + (lane >> 3)) * D + cbd * 64 + (lane & 7) * 8) * 2; step = 16 * D; n = 14; n0 = 0; }
        else if (role == 3) { sb = PTR(ka, 7); vo = ((wv * 448 + (lane >> 1)) * D + r * 16 + (lane & 1) * 8) * 2; step = 64 * D; n = 14; n0 = 0; }
#pragma unroll 1
        for (int s = 0; s < n0; ++s) glds16(vo + s * step, sb, lbase + L_ROLE + (wv * nw + s) * 1024);
      }
      int sto = (unsigned)(lane - 4) < 8u ? (cu >> 6) * BLK + (cu & 63) * 128 + (lane - 4) * 16 : WS_END;
      PIN(sto);
      const unsigned tag16 = epoch & 0xFFFFu;
      BAR();
      if (wv == 0) {
        const float mw = LDS(float, L_ML + c * 4), lw = LDS(float, L_ML + 64 + c * 4);
        float M = fmax2(mw, dpp<0xB1>(mw)); M = fmax2(M, dpp<0x4E>(M)); M = fmax2(M, dpp<0x141>(M));
        float ls = __builtin_amdgcn_exp2f(mw - M) * lw;
        ls += dpp<0xB1>(ls); ls += dpp<0x4E>(ls); ls += dpp<0x141>(ls);
        const v4u rec = {(uf(M) & 0xFFFF0000u) | tag16, (uf(M) << 16) | tag16, (uf(ls) & 0xFFFF0000u) | tag16, (uf(ls) << 16) | tag16};
        ST128(rec, sto, lo + WS_ST + (P0 ^ lx));
      }
      if (n > n0) {
        BAR();
#pragma unroll 1
        for (int s = n0; s < n; ++s) {
          const bool second = role == 2 && s >= 7;
          glds16(vo + (second ? s - 7 : s) * step, second ? PTR(ka, 6) : sb, lbase + L_ROLE + (wv * nw + (second ? s + 21 : s)) * 1024);
        }
      }
      if (role == 1) {
        if (wv == 0) {
          SLEEP(DST);
          const int sa = (go >> 1) * BLK + ((go & 1) * 32 + (lane & 31)) * 128 + hh * 64;
          v4u st;
          unsigned sp = 0;
          POLL {
            st = LD128(sa, lo + WS_ST + (P0 ^ lx));
            st ^= tag16;
            DONE_IF(all_zero(or4u(st, 0u) & 0xFFFFu));
          }
          CNT(46 + layer * 8, sp);
          const float ms = fu(st.x | (st.y >> 16)), lsum = fu(st.z | (st.w >> 16));
          float M = row_max16(ms); M = swap16_max(M);
          const float e = __builtin_amdgcn_exp2f(ms - M);
          float Ls = row_sum16(e * lsum); Ls = swap16_add(Ls, Ls);
          LDS(float, L_WT + (lane & 7) * 16 + (lane >> 3 & 3) * 4) = e * __builtin_amdgcn_rcpf(Ls);
          STAMP(4 + layer * 16);
        }
        VMWAIT();
        BAR();
        v4u W[8], A[8];
#pragma unroll
        for (int s = 0; s < 8; ++s) W[s] = trfrag(L_ROLE + s * 4096 + trrow * 128 + wv * 32 + trp * 8, 512);
        int so = lane < 16 ? (h >> 2) * BLK + (h & 3) * 2048 + cbo * 256 + wv * 64 + lane * 4 : WS_END, ao = L_O + G * 16;
        PIN(so); PIN(ao);
        BAR();
#pragma unroll
        for (int s = 0; s < 8; ++s) A[s] = LDS(v4u, ao + s * 64);
        FENCE();
        f32x4 acc = {0, 0, 0, 0};
#pragma unroll
        for (int s = 0; s < 8; ++s) acc = mfma(A[s], W[s], acc);
        ST32(NOSENT(uf(acc[0])), so, lo + WS_X1P + (P1 ^ lx));
        STAMP(8 + layer * 16);
      } else if (role == 2) {
        if (layer == 0 && wv == 3) check_flags();
        VMWAIT();
        STAMP(14 + layer * 16);
        BAR();
        v4u W[14], A[14];
        f32x4 a = {0, 0, 0, 0};
#pragma unroll
        for (int s = 0; s < 14; ++s) {
          W[s] = trfrag(L_ROLE + s * 2048 + trrow * 64 + (trp >> 1) * 28672 + wv * 16 + (trp & 1) * 8, 256);
          a = mfma(ones, W[s], a);
        }
        int ho = wv * 64 + c * 4, ao = L_X1 + G * 16;
        LDS(float, L_CS + ho) = a[0];
        PIN(ho); PIN(ao);
        BAR();
#pragma unroll
        for (int s = 0; s < 14; ++s) A[s] = LDS(v4u, ao + s * 64);
        FENCE();
        f32x4 acc = {0, 0, 0, 0};
#pragma unroll
        for (int s = 0; s < 14; ++s) acc = mfma(A[s], W[s], acc);
        LDS(float, L_HM + ho) = acc[0];
        BAR();
      } else if (role == 4) {
        if (layer == 0) {
          check_flags();
          STAMP(15);
          BAR();
          v4u W[14];
#pragma unroll
          for (int s = 0; s < 14; ++s) W[s] = trfrag(L_ROLE + s * 4096 + trrow * 128 + wv * 32 + trp * 8, 512);
          const int ha = c < 14 ? (kb * 14 + c) * 128 + G * 16 : WS_END;
          const int xa = kb == 0 ? (cbd * 8 + wv * 2 + (c >> 3)) * 128 + 64 + ((c & 7) >> 1) * 4 : WS_END;
          int so = lane < 32 ? G * 8192 + kb * 2048 + cbd * 256 + wv * 64 + c * 4 : WS_END;
          unsigned sh = (c & 1) ? 0u : 16u;
          PIN(so); PIN(sh);
          v4u hc; unsigned x1w;
          unsigned sp = 0;
          POLL {
            hc = LD128(ha, WS_H + P1); x1w = LD32(xa, WS_H + P1);
            DONE_IF(none_eq(max4u(hc, x1w), SENT));
          }
          CNT(43, sp);
          STAMP(11);
          f32x4 acc = {fu((x1w << sh) & 0xFFFF0000u), 0, 0, 0};
#define BC(s) (v4u){(unsigned)__builtin_amdgcn_mov_dpp((int)hc.x, 0x150 + s, 0xf, 0xf, false), (unsigned)__builtin_amdgcn_mov_dpp((int)hc.y, 0x150 + s, 0xf, 0xf, false), \
                    (unsigned)__builtin_amdgcn_mov_dpp((int)hc.z, 0x150 + s, 0xf, 0xf, false), (unsigned)__builtin_amdgcn_mov_dpp((int)hc.w, 0x150 + s, 0xf, 0xf, false)}
          acc = mfma(BC(0), W[0], acc); acc = mfma(BC(1), W[1], acc); acc = mfma(BC(2), W[2], acc); acc = mfma(BC(3), W[3], acc);
          acc = mfma(BC(4), W[4], acc); acc = mfma(BC(5), W[5], acc); acc = mfma(BC(6), W[6], acc); acc = mfma(BC(7), W[7], acc);
          acc = mfma(BC(8), W[8], acc); acc = mfma(BC(9), W[9], acc); acc = mfma(BC(10), W[10], acc); acc = mfma(BC(11), W[11], acc);
          acc = mfma(BC(12), W[12], acc); acc = mfma(BC(13), W[13], acc);
          ST32(NOSENT(uf(acc[0])), so, WS_X2P);
          STAMP(12);
        }
      } else if (role == 3) {
        if (layer == 0) {
          VMWAIT();
          STAMP(16);
        } else {
          unsigned x1w = 0;
          if (wv == 0) {
            SLEEP(D4);
            const int xa = (2 * r + (c >> 3)) * 128 + 64 + ((c & 7) >> 1) * 4;
            unsigned sp = 0;
            POLL {
              x1w = LD32(xa, LAY + WS_H + (P1 ^ 8192));
              DONE_IF(none_eq(x1w, SENT));
            }
            x1w = (x1w << ((c & 1) ? 0 : 16)) & 0xFFFF0000u;
            PIN(x1w);
          }
          BAR();
          if (wv == 0) {
            const f32x4 pa = LDS(f32x4, L_DP + c * 32), pb = LDS(f32x4, L_DP + c * 32 + 16);
            const float y = ((pa[0] + pa[1]) + (pa[2] + pa[3])) + ((pb[0] + pb[1]) + (pb[2] + pb[3]));
            const float x2 = fu(x1w) + rbf(y);
            if (lane < 16) ((GAS u16*)PTR(kb4, 0))[r * 16 + lane] = (u16)pkbf(x2, x2);
            ST32(epoch, (r | lane) == 0 ? WS_EPOCH : WS_END, 0);
            STAMP(28);
          }
        }
      }
      if (layer == 1) break;
      if (wv < 2) {
        const int qa = wv * 1024 + lane * 16, qs = WS_CS + g * BLK + PG;
        unsigned sp = 0;
        POLL {
          v4u a0 = LD128(qa, qs), a1 = LD128(qa + 2048, qs), a2 = LD128(qa, qs + 4096), a3 = LD128(qa + 2048, qs + 4096);
          a0 ^= tag12; a1 ^= tag12; a2 ^= tag12; a3 ^= tag12;
          const unsigned bad = or4u(a3, or4u(a2, or4u(a1, or4u(a0, 0u)))) & 0xFFFu;
#pragma unroll
          for (int i = 0; i < 4; ++i) cst[i] = ((fu(a0[i]) + fu(a1[i])) + fu(a2[i])) + fu(a3[i]);
          DONE_IF(all_zero(bad));
        }
        const v4u zz = {0, 0, 0, 0};
        LDS(v4u, L_X + 896) = zz;
      }
      mup = 0.f;
      load_wq();
      BAR();
      xc = LDS(v4u, xoff);
    }
    FLUSH_STAMPS();
    __builtin_amdgcn_endpgm();
  }
  {
    const int wave = wv - 4, lane = lane_, c = lane & 15, G = lane >> 4;
    v4u K0, K1, K2, K3, K4, K5, K6, K7, V0, V1, V2, V3, V4, V5, V6, V7;
    {
      const unsigned kvo = (unsigned)(g * SK + (SK - WIN) + r * 128 + wave * 16) * 512u;
      const u64 kbp = PTR(ka, 1) + kvo, vb = PTR(ka, 2) + kvo;
      const unsigned ko = c * 512 + G * 16, vo = G * 2048 + c * 16;
      KVLD(K0, ko, kbp, 0); THR(1); KVLD(K1, ko, kbp, 64); THR(2); KVLD(K2, ko, kbp, 128); THR(3); KVLD(K3, ko, kbp, 192); THR(4);
      KVLD(K4, ko, kbp, 256); THR(5); KVLD(K5, ko, kbp, 320); THR(6); KVLD(K6, ko, kbp, 384); THR(7); KVLD(K7, ko, kbp, 448); THR(8);
      KVLD(V0, vo, vb, 0); THR(9); KVLD(V1, vo, vb, 256); THR(10); KVLD(V2, vo, vb, 512); THR(11); KVLD(V3, vo, vb, 768); THR(12);
      KVLD(V4, vo, vb, 1024); THR(13); KVLD(V5, vo, vb, 1280); THR(14); KVLD(V6, vo, vb, 1536); THR(15); KVLD(V7, vo, vb, 1792);
    }
    load_epoch();
    STAMP(1);
    unsigned pm[4];
#pragma unroll
    for (int q = 0; q < 4; ++q) pm[q] = (((c & 7) >> 1) == q) ? ((c & 1) ? 0xFFFF0000u : 0x0000FFFFu) : 0u;
    const int col = wave * 64 + c * 4 + G;

#pragma unroll 1
    for (int layer = 0; layer < 2; ++layer) {
      const int lo = layer * LAY, lx = layer * 8192;
      BAR();
      STAMP(32 + layer);
      {
        const int qo = L_Q + (c >> 3) * QSTR + G * 16;
        const v4u q0 = LDS(v4u, qo), q1 = LDS(v4u, qo + 64), q2 = LDS(v4u, qo + 128), q3 = LDS(v4u, qo + 192),
                  q4 = LDS(v4u, qo + 256), q5 = LDS(v4u, qo + 320), q6 = LDS(v4u, qo + 384), q7 = LDS(v4u, qo + 448);
        const float SC = LDS(float, L_SC);
        FENCE();
        asm volatile("s_waitcnt vmcnt(8)" : "+v"(K0), "+v"(K1), "+v"(K2), "+v"(K3), "+v"(K4), "+v"(K5), "+v"(K6), "+v"(K7));
        f32x4 s = {0, 0, 0, 0};
        s = mfma(K0, q0, s); s = mfma(K1, q1, s); s = mfma(K2, q2, s); s = mfma(K3, q3, s);
        s = mfma(K4, q4, s); s = mfma(K5, q5, s); s = mfma(K6, q6, s); s = mfma(K7, q7, s);
        float m = fmax2(fmax2(s[0], s[1]), fmax2(s[2], s[3]));
        m = swap16_max(m); m = swap32_max(m);
        m *= SC;
        float p0 = __builtin_amdgcn_exp2f(s[0] * SC - m), p1 = __builtin_amdgcn_exp2f(s[1] * SC - m),
              p2 = __builtin_amdgcn_exp2f(s[2] * SC - m), p3 = __builtin_amdgcn_exp2f(s[3] * SC - m);
        asm volatile("s_waitcnt vmcnt(0)" : "+v"(V0), "+v"(V1), "+v"(V2), "+v"(V3), "+v"(V4), "+v"(V5), "+v"(V6), "+v"(V7), "+v"(p0));
        f32x4 o0 = {0, 0, 0, 0}, o1 = {0, 0, 0, 0};
        unsigned pb; v4u pf;
        pb = pkbf(p0, p0); pf = (v4u){pb & pm[0], pb & pm[1], pb & pm[2], pb & pm[3]};
        o0 = mfma(V0, pf, o0); o1 = mfma(V1, pf, o1);
        pb = pkbf(p1, p1); pf = (v4u){pb & pm[0], pb & pm[1], pb & pm[2], pb & pm[3]};
        o0 = mfma(V2, pf, o0); o1 = mfma(V3, pf, o1);
        pb = pkbf(p2, p2); pf = (v4u){pb & pm[0], pb & pm[1], pb & pm[2], pb & pm[3]};
        o0 = mfma(V4, pf, o0); o1 = mfma(V5, pf, o1);
        pb = pkbf(p3, p3); pf = (v4u){pb & pm[0], pb & pm[1], pb & pm[2], pb & pm[3]};
        o0 = mfma(V6, pf, o0); o1 = mfma(V7, pf, o1);
        float l = (p0 + p1) + (p2 + p3);
        l = swap16_add(l, l); l = swap32_add(l, l);
        LDS(float, L_ML + (c >> 3) * 32 + wave * 4) = m;
        LDS(float, L_ML + 64 + (c >> 3) * 32 + wave * 4) = l;
        const int so = L_SLAB + wave * 2048 + (c >> 3) * 1024 + (G * 8 + (c & 7)) * 16;
        LDS(f32x4, so) = o0;
        LDS(f32x4, so + 512) = o1;
        STAMP(5 + layer * 16);
      }
      BAR();
      STAMP(34 + layer);
      {
        const int t4 = (wave * 64 + lane) * 4;
        const float mw = LDS(float, L_ML + (wave >> 2) * 32 + (c & 7) * 4);
        float ov[8];
#pragma unroll
        for (int w = 0; w < 8; ++w) ov[w] = LDS(float, L_SLAB + w * 2048 + t4);
        FENCE();
        float M = fmax2(mw, dpp<0xB1>(mw)); M = fmax2(M, dpp<0x4E>(M)); M = fmax2(M, dpp<0x141>(M));
        const float e = __builtin_amdgcn_exp2f(mw - M);
        float o = dpp<0x150>(e) * ov[0];
        o += dpp<0x151>(e) * ov[1]; o += dpp<0x152>(e) * ov[2]; o += dpp<0x153>(e) * ov[3];
        o += dpp<0x154>(e) * ov[4]; o += dpp<0x155>(e) * ov[5]; o += dpp<0x156>(e) * ov[6]; o += dpp<0x157>(e) * ov[7];
        ST32(TAGGED(o), t4, lo + WS_PO + (cu >> 2) * BLK + (cu & 3) * 2048 + (P0 ^ lx));
      }
      STAMP(6 + layer * 16);
      if (role == 1) {
        SLEEP(D2);
        const int a = lane >> 3, c8 = lane & 7;
        const int pb = (a >> 2) * BLK + (a & 3) * 2048 + wave * 128 + c8 * 16, ps = lo + WS_PO + go * 8 * BLK + hh * 1024 + (P0 ^ lx);
        v4u p0 = LD128(pb, ps), p1 = LD128(pb, ps + 2 * BLK), p2 = LD128(pb, ps + 4 * BLK), p3 = LD128(pb, ps + 6 * BLK);
        BAR();
        const f32x4 wt = LDS(f32x4, L_WT + a * 16);
        unsigned sp = 0;
        float o;
        POLL {
          p0 ^= tag12; p1 ^= tag12; p2 ^= tag12; p3 ^= tag12;
          float v[4];
#pragma unroll
          for (int i = 0; i < 4; ++i) v[i] = ((wt[0] * fu(p0[i]) + wt[1] * fu(p1[i])) + wt[2] * fu(p2[i])) + wt[3] * fu(p3[i]);
          const unsigned bad = or4u(p3, or4u(p2, or4u(p1, or4u(p0, 0u)))) & 0xFFFu;
          o = swap16_add(swap32_add(v[0], v[2]), swap32_add(v[1], v[3]));
          o += dpp<0x128>(o);
          DONE_IF(all_zero(bad));
          p0 = LD128(pb, ps); p1 = LD128(pb, ps + 2 * BLK); p2 = LD128(pb, ps + 4 * BLK); p3 = LD128(pb, ps + 6 * BLK);
        }
        LDS(u16, L_O + (wave * 32 + G * 8 + c8) * 2) = (u16)pkbf(o, o);
        CNT(41 + layer * 8, sp);
        STAMP(7 + layer * 16);
        BAR();
      } else if (role == 2) {
        if (layer == 0) BAR();
        BAR();
        if (wave < 7) {
          if (layer) SLEEP(D3);
          const int xb = G * 2048 + wave * 256 + c * 16, xs = lo + WS_X1P + (P1 ^ lx);
          const float xres = fu((unsigned)LDS(u16, L_X + col * 2) << 16);
          unsigned sp = 0;
          float x1;
          POLL {
            const v4u p0 = LD128(xb, xs), p1 = LD128(xb, xs + BLK), p2 = LD128(xb, xs + 2 * BLK), p3 = LD128(xb, xs + 3 * BLK);
            float v[4];
#pragma unroll
            for (int i = 0; i < 4; ++i) v[i] = ((fu(p0[i]) + fu(p1[i])) + fu(p2[i])) + fu(p3[i]);
            const unsigned mx = max4u(p3, max4u(p2, max4u(p1, max4u(p0, 0u))));
            x1 = xres + rbf(swap16_add(swap32_add(v[0], v[2]), swap32_add(v[1], v[3])));
            DONE_IF(none_eq(mx, SENT));
          }
          LDS(u16, L_X1 + col * 2) = (u16)pkbf(x1, x1);
          CNT(42 + layer * 8, sp);
          STAMP(9 + layer * 16);
          BAR();
          BAR();
        } else {
          int so = (c & 9) == 0 ? G * 16 + c * 2 : ((lane & 0x39) == 8 ? 64 + (c - 8) * 2 : WS_END);
          PIN(so);
          BAR();
          v4u x1c = LDS(v4u, L_X1 + lane * 16);
          const float cs = LDS(float, L_CS + lane * 4);
          const unsigned xw = LDS(unsigned, L_X1 + idg * 16 + (c & 6) * 2);
          if (lane >= 56) x1c = (v4u){0, 0, 0, 0};
          float mu, rstd;
          ln_stats(x1c, mu, rstd);
          float off = -mu * cs * rstd;
          PIN(off);
          BAR();
          const float raw = LDS(float, L_HM + lane * 4);
          const float gu = rbf(raw * rstd + off);
          const float sg = rbf(gu * __builtin_amdgcn_rcpf(1.f + __builtin_amdgcn_exp2f(-1.4426950408889634f * gu)));
          const float hv = sg * dpp<0x128>(gu);
          const unsigned hp = pkbf(hv, dpp<0xB1>(hv));
          ST32(NOSENT(c < 8 ? hp : xw), so, lo + WS_H + idg * 128 + (P1 ^ lx));
          STAMP(10 + layer * 16);
        }
      } else if (role == 4) {
        if (layer == 0) { BAR(); BAR(); }
      } else if (role == 3) {
        if (layer == 0) {
          BAR();
        } else {
          v4u W[7];
#pragma unroll
          for (int s = 0; s < 7; ++s) W[s] = trfrag(L_ROLE + wave * 7168 + s * 1024 + (8 * G + (c >> 2)) * 32 + (c & 3) * 8, 128);
          SLEEP(D4);
          const int hb = wave * 896 + G * 16;
          unsigned sp = 0;
          f32x4 acc;
          POLL {
            v4u hv[7];
            unsigned mx = 0;
#pragma unroll
            for (int s = 0; s < 7; ++s) hv[s] = LD128(hb + s * 128, LAY + WS_H + (P1 ^ 8192));
            FENCE();
            acc = (f32x4){0, 0, 0, 0};
#pragma unroll
            for (int s = 0; s < 7; ++s) { acc = mfma(hv[s], W[s], acc); mx = max4u(hv[s], mx); }
            DONE_IF(none_eq(mx, SENT));
          }
          LDS(float, L_DP + c * 32 + wave * 4) = acc[0];
          CNT(43 + layer * 8, sp);
          STAMP(11 + layer * 16);
          BAR();
        }
      }
      if (layer == 1) break;
      if (wave < 7) {
        check_flags();
        if (role == 0 || role == 3) SLEEP(D5);
        if (role == 1 || role == 4) SLEEP(D5 / 2);
        const int xb = G * 2048 + wave * 256 + c * 16;
        unsigned sp = 0;
        float x2;
        POLL {
          const v4u p = LD128(xb, WS_X2P + PG);
          x2 = swap16_add(swap32_add(fu(p[0]), fu(p[2])), swap32_add(fu(p[1]), fu(p[3])));
          DONE_IF(none_eq(max4u(p, 0u), SENT));
        }
        LDS(u16, L_X + col * 2) = (u16)pkbf(x2, x2);
        CNT(44, sp);
        STAMP(13);
      }
      BAR();
    }
    FLUSH_STAMPS();
  }
}

extern "C" void kernel_launch(void* const* d_in, const int* in_sizes, int n_in, void* d_out, int out_size, void* d_ws,
                              size_t ws_size, hipStream_t stream) {
  fused_decode<<<256, 768, 0, stream>>>((const u16*)d_in[0], (const u16*)d_in[1], (const u16*)d_in[2], (const u16*)d_in[3],
                                        (const u16*)d_in[4], (const u16*)d_in[6], (const u16*)d_in[7], (const u16*)d_in[8],
                                        (unsigned*)d_out, (unsigned char*)d_ws);
}
```

```cpp
#define DIAG 99
#define DCLS -1
#include <hip/hip_runtime.h>
#include <stdint.h>

typedef unsigned short u16;
typedef unsigned long long u64;
typedef unsigned v4u __attribute__((ext_vector_type(4)));
typedef unsigned v2u __attribute__((ext_vector_type(2)));
typedef unsigned v16u __attribute__((ext_vector_type(16)));
typedef short v4s __attribute__((ext_vector_type(4)));
typedef float f32x4 __attribute__((ext_vector_type(4)));
typedef float f32x2 __attribute__((ext_vector_type(2)));
typedef __bf16 bf16x8 __attribute__((ext_vector_type(8)));
typedef __bf16 bf16x2 __attribute__((ext_vector_type(2)));
#define LAS __attribute__((address_space(3)))
#define GAS __attribute__((address_space(1)))

constexpr int D = 448, SK = 65536, WIN = 4096, FF = 1792;

constexpr int LAY = 2 << 20, BLK = 16384;
constexpr int WS_EPOCH = 0;
constexpr int WS_FLAG = 2048;
constexpr int WS_X2P = 1 * BLK;
constexpr int WS_H = 2 * BLK;
constexpr int WS_X1P = 3 * BLK;
constexpr int WS_ST = 7 * BLK;
constexpr int WS_QP = 11 * BLK;
constexpr int WS_CS = 19 * BLK;
constexpr int WS_PO = 27 * BLK;
constexpr int WS_END = 2 * LAY;
constexpr int WS_STAMP = 4 << 20;
constexpr int L_ML = 0, L_WT = 128, L_SC = 256, L_CS = 384, L_HM = 640, L_DP = 896, L_O = 1408, L_Q = 1920, QSTR = 576, L_X = L_Q + 1152,
              L_X1 = L_X + 1024, L_SLAB = L_X1 + 1024, L_WQ = L_SLAB + 16384, L_ROLE = L_WQ + 16384, L_END = L_ROLE + 57344;

#define CPAR 0
#define DQ 4
#define D2 4
#define DST 4
#define D3 30
#define D4 60
#define D5 80
#define TH 0
#define TQ0 8000
#define PSL 1
#define CODE_LINES 96
constexpr unsigned SPIN_CAP = 1u << 17;
constexpr unsigned SENT = 0xFFFFFFFFu;

__device__ __forceinline__ f32x4 mfma(v4u a, v4u b, f32x4 c) {
  return __builtin_amdgcn_mfma_f32_16x16x32_bf16(__builtin_bit_cast(bf16x8, a), __builtin_bit_cast(bf16x8, b), c, 0, 0, 0);
}
__device__ __forceinline__ float dot2(unsigned a, unsigned b, float c) {
  return __builtin_amdgcn_fdot2_f32_bf16(__builtin_bit_cast(bf16x2, a), __builtin_bit_cast(bf16x2, b), c, false);
}
__device__ __forceinline__ unsigned pkbf(float lo, float hi) {
  f32x2 f = {lo, hi};
  return __builtin_bit_cast(unsigned, __builtin_convertvector(f, bf16x2));
}
__device__ __forceinline__ float fu(unsigned u) { return __builtin_bit_cast(float, u); }
__device__ __forceinline__ unsigned uf(float f) { return __builtin_bit_cast(unsigned, f); }
__device__ __forceinline__ float rbf(float f) { return fu(pkbf(f, f) & 0xFFFF0000u); }
template <int CTRL> __device__ __forceinline__ float dpp(float x) {
  return __builtin_bit_cast(float, __builtin_amdgcn_mov_dpp(__builtin_bit_cast(int, x), CTRL, 0xf, 0xf, true));
}
__device__ __forceinline__ float fmax2(float a, float b) { return __builtin_fmaxf(a, b); }
__device__ __forceinline__ float swap32_add(float a, float b) {
  auto s = __builtin_amdgcn_permlane32_swap(uf(a), uf(b), false, false);
  return fu(s[0]) + fu(s[1]);
}
__device__ __forceinline__ float swap16_add(float a, float b) {
  auto s = __builtin_amdgcn_permlane16_swap(uf(a), uf(b), false, false);
  return fu(s[0]) + fu(s[1]);
}
__device__ __forceinline__ float swap32_max(float a) {
  auto s = __builtin_amdgcn_permlane32_swap(uf(a), uf(a), false, false);
  return fmax2(fu(s[0]), fu(s[1]));
}
__device__ __forceinline__ float swap16_max(float a) {
  auto s = __builtin_amdgcn_permlane16_swap(uf(a), uf(a), false, false);
  return fmax2(fu(s[0]), fu(s[1]));
}
__device__ __forceinline__ float row_sum16(float v) {
  v += dpp<0x128>(v); v += dpp<0x124>(v); v += dpp<0x122>(v); v += dpp<0x121>(v);
  return v;
}
__device__ __forceinline__ float row_max16(float v) {
  v = fmax2(v, dpp<0x128>(v)); v = fmax2(v, dpp<0x124>(v)); v = fmax2(v, dpp<0x122>(v)); v = fmax2(v, dpp<0x121>(v));
  return v;
}
__device__ __forceinline__ unsigned umax(unsigned a, unsigned b) { return a > b ? a : b; }
__device__ __forceinline__ unsigned umin(unsigned a, unsigned b) { return a < b ? a : b; }
__device__ __forceinline__ unsigned max4u(v4u v, unsigned acc) { return umax(umax(umax(umax(acc, v.x), v.y), v.z), v.w); }
__device__ __forceinline__ unsigned or4u(v4u v, unsigned acc) { return (acc | v.x | v.y) | v.z | v.w; }
__device__ __forceinline__ bool none_eq(unsigned v, unsigned x) { return __builtin_amdgcn_ballot_w64(v == x) == 0ull; }
__device__ __forceinline__ bool all_zero(unsigned v) { return __builtin_amdgcn_ballot_w64(v != 0u) == 0ull; }
__device__ __forceinline__ void glds16(unsigned voff, u64 sbase, unsigned lds_dst) {
  unsigned keep;
  asm volatile("s_mov_b32 %0, m0\n\ts_mov_b32 m0, %3\n\ts_nop 0\n\tglobal_load_lds_dwordx4 %1, %2 nt\n\ts_mov_b32 m0, %0"
               : "=&s"(keep) : "v"(voff), "s"(sbase), "s"(lds_dst) : "memory");
}
__device__ __forceinline__ void ln_stats(v4u x, float& mu, float& rstd) {
  const unsigned one2 = 0x3F803F80u;
  float s1 = dot2(x.x, one2, 0.f), s2 = dot2(x.x, x.x, 0.f);
  s1 = dot2(x.y, one2, s1); s2 = dot2(x.y, x.y, s2);
  s1 = dot2(x.z, one2, s1); s2 = dot2(x.z, x.z, s2);
  s1 = dot2(x.w, one2, s1); s2 = dot2(x.w, x.w, s2);
  float t = swap32_add(s1, s2);
  t = swap16_add(t, t);
  t = row_sum16(t);
  const float S1 = fu(__builtin_amdgcn_readlane(uf(t), 0)), S2 = fu(__builtin_amdgcn_readlane(uf(t), 32));
  const float mean = S1 * (1.f / D);
  const float var = fmax2(S2 * (1.f / D) - mean * mean, 0.f);
  mu = rbf(mean);
  const float den = rbf(__builtin_amdgcn_sqrtf(rbf(rbf(var) + 1e-5f)));
  rstd = __builtin_amdgcn_rcpf(den);
}
#define KVLD(dst, voff, sbase, imm) \
  asm volatile("global_load_dwordx4 %0, %1, %2 offset:" #imm " nt" : "=v"(dst) : "v"(voff), "s"(sbase) : "memory")
#define THR(i) do { if (TH && (i) >= TH) asm volatile("s_waitcnt vmcnt(%0)" :: "i"(TH > 0 ? TH - 1 : 0) : "memory"); } while (0)
#define BAR() do { asm volatile("s_waitcnt lgkmcnt(0)" ::: "memory"); __builtin_amdgcn_s_barrier(); asm volatile("" ::: "memory"); } while (0)
#define VMWAIT() asm volatile("s_waitcnt vmcnt(0)" ::: "memory")
#define FENCE() __builtin_amdgcn_sched_barrier(0)
#define PIN(x) asm volatile("" : "+v"(x))
#define LD128(off, so) __builtin_amdgcn_raw_buffer_load_b128(rs, (off), (so), 16)
#define LD32(off, so) __builtin_amdgcn_raw_buffer_load_b32(rs, (off), (so), 16)
#define ST128(v, off, so) __builtin_amdgcn_raw_buffer_store_b128((v), rs, (off), (so), 16)
#define ST64(v, off, so) __builtin_amdgcn_raw_buffer_store_b64((v), rs, (off), (so), 16)
#define ST32(v, off, so) __builtin_amdgcn_raw_buffer_store_b32((v), rs, (off), (so), 16)
#define SLEEP(n) do { if ((n) > 0) __builtin_amdgcn_s_sleep(n); } while (0)
#define POLL _Pragma("clang loop unroll(disable)") for (;;)
#define DONE_IF(cond) if (__builtin_expect((cond), 1) || ++sp > SPIN_CAP) break; __builtin_amdgcn_s_sleep(PSL)
#define LDS(T, off) (*(LAS T*)(L + (off)))
#define NOSENT(u) umin((u), 0xFFFFFFFEu)
#define TAGGED(f) ((uf(f) & 0xFFFFF000u) | tag12)

#define RSTAMP(k) do {} while (0)
#define STAMP(k) do { if ((k) == DIAG && (DCLS < 0 || (wv >= 4) == DCLS)) tev = __builtin_amdgcn_s_memrealtime(); } while (0)
#define CNT(k, v) do {} while (0)
#define FLUSH_STAMPS() do { const u64 tgt = 6000 + 4 * (tev - tz); while (__builtin_amdgcn_s_memrealtime() - tz < tgt) __builtin_amdgcn_s_sleep(1); } while (0)

__global__ void __launch_bounds__(768)
fused_decode(const u16* xin_, const u16* kc_, const u16* vc_, const u16* wq_, const u16* wo_, const u16* wg_, const u16* wu_,
             const u16* wd_, unsigned* out_, unsigned char* ws_) {
  __shared__ __attribute__((aligned(16))) unsigned char smem[L_END];
  LAS unsigned char* L = (LAS unsigned char*)smem;
  const unsigned lbase = (unsigned)(uintptr_t)L;
  const u64 tz = __builtin_amdgcn_s_memrealtime(); u64 tev = tz;
  v16u ka; v4u kb4; u64 pc0;
  asm volatile("s_load_dwordx16 %0, %3, 0x0\n\ts_load_dwordx4 %1, %3, 0x40\n\ts_getpc_b64 %2"
               : "=&s"(ka), "=&s"(kb4), "=&s"(pc0) : "s"(__builtin_amdgcn_kernarg_segment_ptr()) : "memory");
  const int tid = threadIdx.x, lane_ = tid & 63, wv = __builtin_amdgcn_readfirstlane(tid >> 6);
  const int b = blockIdx.x, g = b & 7, r = b >> 3, cu = g * 32 + r;
  const int gq = g ^ 1, rb = r >> 3, cbq = r & 7, s0 = rb * 4 - (rb == 3);
  const bool svc = wv < 4;
  if (svc) __builtin_amdgcn_s_setprio(3); else if (wv < 8) __builtin_amdgcn_s_setprio(1);
  asm volatile("s_waitcnt lgkmcnt(0)" : "+s"(ka), "+s"(kb4));
  STAMP(0); RSTAMP(60);
#define PTR(v, i) ((u64)(v)[2 * (i)] | ((u64)(v)[2 * (i) + 1] << 32))
  if (wv < 2) {
    glds16(umin(lane_, 55) * 16u, PTR(ka, 0), lbase + L_X);
    u64 src = PTR(ka, 3) + (unsigned)(s0 * 32 * 8192 + (gq * 512 + cbq * 64 + wv * 32) * 2);
    const unsigned vo = (unsigned)(lane_ >> 2) * 8192u + (lane_ & 3) * 16u;
    const int n = rb >= 2 ? 6 : 8;
#pragma unroll 1
    for (int s = 0; s < n; ++s) { glds16(vo, src, lbase + L_WQ + wv * 8192 + s * 1024); src += 131072; }
  }
  const u64 wsp = PTR(kb4, 1);
  const __amdgpu_buffer_rsrc_t rs = __builtin_amdgcn_make_buffer_rsrc((void*)wsp, 0, WS_END, 0x00020000);
  const int role = r < 28 ? (int)((0x34221111u >> (g * 4)) & 7) : 0;
  const int q7 = (r * 37) >> 8, cbo = r - 7 * q7;
  const int go = (q7 & 2) ? 4 + g : (g ^ 1), hh = q7 & 1, h = 2 * go + hh;
  const int idg = (g - 4) * 28 + r, kb = r & 3, cbd = r >> 2;
  const int P0 = CPAR * 8192, P1 = 8192 - P0, PG = g < 4 ? P0 : P1;
  auto trfrag = [&](int addr, int step4) -> v4u {
    v4s lo = __builtin_amdgcn_ds_read_tr16_b64_v4i16((LAS v4s*)(L + addr));
    v4s hi = __builtin_amdgcn_ds_read_tr16_b64_v4i16((LAS v4s*)(L + addr + step4));
    v2u a = __builtin_bit_cast(v2u, lo), bb = __builtin_bit_cast(v2u, hi);
    return (v4u){a.x, a.y, bb.x, bb.y};
  };
  unsigned epoch, flaghi, tag12;
  auto load_epoch = [&]() {
    asm volatile("s_load_dword %0, %1, 0x0\n\ts_waitcnt lgkmcnt(0)" : "=s"(epoch) : "s"(wsp) : "memory");
    epoch += 1u;
    flaghi = epoch * 0x9E3779B1u + 0x7F4A7C15u;
    tag12 = epoch & 0xFFFu;
  };
  auto check_flags = [&]() {
    unsigned sp = 0;
    POLL {
      const v4u f0 = LD128(lane_ * 16, WS_FLAG), f1 = LD128(lane_ * 16, WS_FLAG + 1024);
      const unsigned bad = (f0.x ^ epoch) | (f0.y ^ flaghi) | (f0.z ^ epoch) | (f0.w ^ flaghi) | (f1.x ^ epoch) | (f1.y ^ flaghi) | (f1.z ^ epoch) | (f1.w ^ flaghi);
      DONE_IF(all_zero(bad));
    }
    CNT(45, sp);
  };

  if (svc) {
    const u64 tstart = __builtin_amdgcn_s_memtime();
    const unsigned one2 = 0x3F803F80u;
    const v4u ones = {one2, one2, one2, one2};
    int xoff, wqoff, qsto;
    {
      const int lane = lane_, c = lane & 15, G = lane >> 4;
      int ks = c + s0; ks -= ks >= 14 ? 14 : 0;
      xoff = c < 14 ? L_X + (4 * ks + G) * 16 : L_X + 896;
      wqoff = L_WQ + (wv & 1) * 8192 + (8 * G + (c >> 2)) * 64 + (c & 3) * 8;
      qsto = lane < 32 ? gq * BLK + rb * 2048 + cbq * 256 + wv * 128 + lane * 4 : WS_END;
      if (wv == 2) {
        const int ln = (r & 7) * (CODE_LINES / 8) + lane;
        if (lane < CODE_LINES / 8) { unsigned t = *(const GAS unsigned*)((pc0 & ~127ull) + ln * 128); asm volatile("" :: "v"(t)); }
      }
      load_epoch();
      if (wv == 3) {
        const v4u sv = {SENT, SENT, SENT, SENT};
        const int k = lane >> 3, l = k & 1, k2 = (k >> 1) & 1;
        int o2 = WS_END;
        if (k < 4) {
          if (role == 1) o2 = l * LAY + WS_X1P + (h >> 2) * BLK + (h & 3) * 2048 + cbo * 256 + k2 * 128 + (P1 ^ (l * 8192));
          else if (role == 2 && k < 2) o2 = l * LAY + WS_H + idg * 128 + (P1 ^ (l * 8192));
          else if (role == 4) o2 = WS_X2P + l * 8192 + kb * 2048 + cbd * 256 + k2 * 128;
        }
        ST128(sv, o2 + (lane & 7) * 16, 0);
        VMWAIT();
        const v2u f = {epoch, flaghi};
        ST64(f, lane == 0 ? b * 8 : WS_END, WS_FLAG);
      }
    }
    v4u Wq[8], xc = {0, 0, 0, 0};
    float csq = 0.f, mup = 0.f;
    f32x4 cst = {0, 0, 0, 0};
    auto load_wq = [&]() {
#pragma unroll
      for (int s = 0; s < 4; ++s) { Wq[s] = trfrag(wqoff + s * 2048, 256); Wq[4 + s] = trfrag(wqoff + s * 2048 + 32, 256); }
    };
    if (wv < 2) {
      const v4u zz = {0, 0, 0, 0};
      if (rb >= 2) { LDS(v4u, L_WQ + wv * 8192 + 6144 + lane_ * 16) = zz; LDS(v4u, L_WQ + wv * 8192 + 7168 + lane_ * 16) = zz; }
      VMWAIT();
      STAMP(1);
      load_wq();
      xc = LDS(v4u, xoff);
      if ((lane_ & 15) >= 14) xc = zz;
      f32x4 a0 = {0, 0, 0, 0}, a1 = {0, 0, 0, 0};
#pragma unroll
      for (int s = 0; s < 4; ++s) { a0 = mfma(ones, Wq[s], a0); a1 = mfma(ones, Wq[4 + s], a1); }
      csq = (lane_ & 16) ? a1[0] : a0[0];
      float rs_;
      ln_stats(xc, mup, rs_);
    }
#pragma unroll 1
    for (int layer = 0; layer < 2; ++layer) {
      const int lo = layer * LAY, lx = layer * 8192;
      if (wv < 2) {
        const f32x4 z = {0, 0, 0, 0};
        const f32x4 d0 = mfma(xc, Wq[0], z), e0 = mfma(xc, Wq[4], z), d1 = mfma(xc, Wq[1], z), e1 = mfma(xc, Wq[5], z);
        const f32x4 d2 = mfma(xc, Wq[2], z), e2 = mfma(xc, Wq[6], z), d3 = mfma(xc, Wq[3], z), e3 = mfma(xc, Wq[7], z);
        const float ta = ((d0[0] + d1[1]) + d2[2]) + d3[3], tb = ((e0[0] + e1[1]) + e2[2]) + e3[3];
        const float tq = fu(__builtin_amdgcn_permlane16_swap(uf(ta), uf(tb), false, false)[0]);
        ST32(TAGGED(tq - mup * csq), qsto, lo + WS_QP + (PG ^ lx));
        FENCE();
        STAMP(2 + layer * 16);
        float mu, rstd;
        ln_stats(xc, mu, rstd);
        LDS(float, L_SC) = rstd * (0.0625f * 1.4426950408889634f);
        f32x4 init = cst * -mu;
        PIN(init);
        if (layer == 0) {
          ST32(TAGGED(csq), qsto, WS_CS + PG);
          while (__builtin_amdgcn_s_memtime() - tstart < TQ0) __builtin_amdgcn_s_sleep(2);
        } else {
          SLEEP(DQ);
        }
        const int qa = wv * 1024 + lane_ * 16, qs = lo + WS_QP + g * BLK + (PG ^ lx);
        unsigned sp = 0;
        v2u qf;
        POLL {
          v4u a0 = LD128(qa, qs), a1 = LD128(qa + 2048, qs), a2 = LD128(qa, qs + 4096), a3 = LD128(qa + 2048, qs + 4096);
          a0 ^= tag12; a1 ^= tag12; a2 ^= tag12; a3 ^= tag12;
          const unsigned bad = or4u(a3, or4u(a2, or4u(a1, or4u(a0, 0u)))) & 0xFFFu;
          float v[4];
#pragma unroll
          for (int i = 0; i < 4; ++i) v[i] = (((init[i] + fu(a0[i])) + fu(a1[i])) + fu(a2[i])) + fu(a3[i]);
          qf = (v2u){pkbf(v[0], v[1]), pkbf(v[2], v[3])};
          DONE_IF(all_zero(bad));
        }
        LDS(v2u, L_Q + wv * QSTR + lane_ * 8) = qf;
        CNT(40 + layer * 8, sp);
        STAMP(3 + layer * 16);
      }
      BAR();
      int lane = lane_; PIN(lane);
      const int c = lane & 15, G = lane >> 4, trrow = 8 * G + (c >> 2), trp = c & 3;
      u64 sb = 0; unsigned vo = 0; int step = 0, n = 0, n0 = 0;
      const int nw = role == 1 ? 8 : (role == 2 ? 7 : 14);
      if (layer == 0) {
        if (role == 1) { sb = PTR(ka, 4); vo = ((h * 256 + wv * 64 + (lane >> 3)) * D + cbo * 64 + (lane & 7) * 8) * 2; step = 16 * D; n = 8; n0 = 8; }
        else if (role == 2) { sb = PTR(ka, 5); vo = ((wv * 112 + (lane >> 2)) * FF + idg * 32 + (lane & 3) * 8) * 2; step = 32 * FF; n = 14; n0 = 4; }
        else if (role == 4) { sb = PTR(ka, 7); vo = ((kb * 448 + wv * 112 + (lane >> 3)) * D + cbd * 64 + (lane & 7) * 8) * 2; step = 16 * D; n = 14; n0 = 0; }
        else if (role == 3) { sb = PTR(ka, 7); vo = ((wv * 448 + (lane >> 1)) * D + r * 16 + (lane & 1) * 8) * 2; step = 64 * D; n = 14; n0 = 0; }
#pragma unroll 1
        for (int s = 0; s < n0; ++s) glds16(vo + s * step, sb, lbase + L_ROLE + (wv * nw + s) * 1024);
      }
      int sto = (unsigned)(lane - 4) < 8u ? (cu >> 6) * BLK + (cu & 63) * 128 + (lane - 4) * 16 : WS_END;
      PIN(sto);
      const unsigned tag16 = epoch & 0xFFFFu;
      BAR();
      if (wv == 0) {
        const float mw = LDS(float, L_ML + c * 4), lw = LDS(float, L_ML + 64 + c * 4);
        float M = fmax2(mw, dpp<0xB1>(mw)); M = fmax2(M, dpp<0x4E>(M)); M = fmax2(M, dpp<0x141>(M));
        float ls = __builtin_amdgcn_exp2f(mw - M) * lw;
        ls += dpp<0xB1>(ls); ls += dpp<0x4E>(ls); ls += dpp<0x141>(ls);
        const v4u rec = {(uf(M) & 0xFFFF0000u) | tag16, (uf(M) << 16) | tag16, (uf(ls) & 0xFFFF0000u) | tag16, (uf(ls) << 16) | tag16};
        ST128(rec, sto, lo + WS_ST + (P0 ^ lx));
      }
      if (n > n0) {
        BAR();
#pragma unroll 1
        for (int s = n0; s < n; ++s) {
          const bool second = role == 2 && s >= 7;
          glds16(vo + (second ? s - 7 : s) * step, second ? PTR(ka, 6) : sb, lbase + L_ROLE + (wv * nw + (second ? s + 21 : s)) * 1024);
        }
      }
      if (role == 1) {
        if (wv == 0) {
          SLEEP(DST);
          const int sa = (go >> 1) * BLK + ((go & 1) * 32 + (lane & 31)) * 128 + hh * 64;
          v4u st;
          unsigned sp = 0;
          POLL {
            st = LD128(sa, lo + WS_ST + (P0 ^ lx));
            st ^= tag16;
            DONE_IF(all_zero(or4u(st, 0u) & 0xFFFFu));
          }
          CNT(46 + layer * 8, sp);
          const float ms = fu(st.x | (st.y >> 16)), lsum = fu(st.z | (st.w >> 16));
          float M = row_max16(ms); M = swap16_max(M);
          const float e = __builtin_amdgcn_exp2f(ms - M);
          float Ls = row_sum16(e * lsum); Ls = swap16_add(Ls, Ls);
          LDS(float, L_WT + (lane & 7) * 16 + (lane >> 3 & 3) * 4) = e * __builtin_amdgcn_rcpf(Ls);
          STAMP(4 + layer * 16);
        }
        VMWAIT();
        BAR();
        v4u W[8], A[8];
#pragma unroll
        for (int s = 0; s < 8; ++s) W[s] = trfrag(L_ROLE + s * 4096 + trrow * 128 + wv * 32 + trp * 8, 512);
        int so = lane < 16 ? (h >> 2) * BLK + (h & 3) * 2048 + cbo * 256 + wv * 64 + lane * 4 : WS_END, ao = L_O + G * 16;
        PIN(so); PIN(ao);
        BAR();
#pragma unroll
        for (int s = 0; s < 8; ++s) A[s] = LDS(v4u, ao + s * 64);
        FENCE();
        f32x4 acc = {0, 0, 0, 0};
#pragma unroll
        for (int s = 0; s < 8; ++s) acc = mfma(A[s], W[s], acc);
        ST32(NOSENT(uf(acc[0])), so, lo + WS_X1P + (P1 ^ lx));
        STAMP(8 + layer * 16);
      } else if (role == 2) {
        if (layer == 0 && wv == 3) check_flags();
        VMWAIT();
        STAMP(14 + layer * 16);
        BAR();
        v4u W[14], A[14];
        f32x4 a = {0, 0, 0, 0};
#pragma unroll
        for (int s = 0; s < 14; ++s) {
          W[s] = trfrag(L_ROLE + s * 2048 + trrow * 64 + (trp >> 1) * 28672 + wv * 16 + (trp & 1) * 8, 256);
          a = mfma(ones, W[s], a);
        }
        int ho = wv * 64 + c * 4, ao = L_X1 + G * 16;
        LDS(float, L_CS + ho) = a[0];
        PIN(ho); PIN(ao);
        BAR();
#pragma unroll
        for (int s = 0; s < 14; ++s) A[s] = LDS(v4u, ao + s * 64);
        FENCE();
        f32x4 acc = {0, 0, 0, 0};
#pragma unroll
        for (int s = 0; s < 14; ++s) acc = mfma(A[s], W[s], acc);
        LDS(float, L_HM + ho) = acc[0];
        BAR();
      } else if (role == 4) {
        if (layer == 0) {
          check_flags();
          STAMP(15);
          BAR();
          v4u W[14];
#pragma unroll
          for (int s = 0; s < 14; ++s) W[s] = trfrag(L_ROLE + s * 4096 + trrow * 128 + wv * 32 + trp * 8, 512);
          const int ha = c < 14 ? (kb * 14 + c) * 128 + G * 16 : WS_END;
          const int xa = kb == 0 ? (cbd * 8 + wv * 2 + (c >> 3)) * 128 + 64 + ((c & 7) >> 1) * 4 : WS_END;
          int so = lane < 32 ? G * 8192 + kb * 2048 + cbd * 256 + wv * 64 + c * 4 : WS_END;
          unsigned sh = (c & 1) ? 0u : 16u;
          PIN(so); PIN(sh);
          v4u hc; unsigned x1w;
          unsigned sp = 0;
          POLL {
            hc = LD128(ha, WS_H + P1); x1w = LD32(xa, WS_H + P1);
            DONE_IF(none_eq(max4u(hc, x1w), SENT));
          }
          CNT(43, sp);
          STAMP(11);
          f32x4 acc = {fu((x1w << sh) & 0xFFFF0000u), 0, 0, 0};
#define BC(s) (v4u){(unsigned)__builtin_amdgcn_mov_dpp((int)hc.x, 0x150 + s, 0xf, 0xf, false), (unsigned)__builtin_amdgcn_mov_dpp((int)hc.y, 0x150 + s, 0xf, 0xf, false), \
                    (unsigned)__builtin_amdgcn_mov_dpp((int)hc.z, 0x150 + s, 0xf, 0xf, false), (unsigned)__builtin_amdgcn_mov_dpp((int)hc.w, 0x150 + s, 0xf, 0xf, false)}
          acc = mfma(BC(0), W[0], acc); acc = mfma(BC(1), W[1], acc); acc = mfma(BC(2), W[2], acc); acc = mfma(BC(3), W[3], acc);
          acc = mfma(BC(4), W[4], acc); acc = mfma(BC(5), W[5], acc); acc = mfma(BC(6), W[6], acc); acc = mfma(BC(7), W[7], acc);
          acc = mfma(BC(8), W[8], acc); acc = mfma(BC(9), W[9], acc); acc = mfma(BC(10), W[10], acc); acc = mfma(BC(11), W[11], acc);
          acc = mfma(BC(12), W[12], acc); acc = mfma(BC(13), W[13], acc);
          ST32(NOSENT(uf(acc[0])), so, WS_X2P);
          STAMP(12);
        }
      } else if (role == 3) {
        if (layer == 0) {
          VMWAIT();
          STAMP(16);
        } else {
          unsigned x1w = 0;
          if (wv == 0) {
            SLEEP(D4);
            const int xa = (2 * r + (c >> 3)) * 128 + 64 + ((c & 7) >> 1) * 4;
            unsigned sp = 0;
            POLL {
              x1w = LD32(xa, LAY + WS_H + (P1 ^ 8192));
              DONE_IF(none_eq(x1w, SENT));
            }
            x1w = (x1w << ((c & 1) ? 0 : 16)) & 0xFFFF0000u;
            PIN(x1w);
          }
          BAR();
          if (wv == 0) {
            const f32x4 pa = LDS(f32x4, L_DP + c * 32), pb = LDS(f32x4, L_DP + c * 32 + 16);
            const float y = ((pa[0] + pa[1]) + (pa[2] + pa[3])) + ((pb[0] + pb[1]) + (pb[2] + pb[3]));
            const float x2 = fu(x1w) + rbf(y);
            if (lane < 16) ((GAS u16*)PTR(kb4, 0))[r * 16 + lane] = (u16)pkbf(x2, x2);
            ST32(epoch, (r | lane) == 0 ? WS_EPOCH : WS_END, 0);
            STAMP(28);
          }
        }
      }
      if (layer == 1) break;
      if (wv < 2) {
        const int qa = wv * 1024 + lane * 16, qs = WS_CS + g * BLK + PG;
        unsigned sp = 0;
        POLL {
          v4u a0 = LD128(qa, qs), a1 = LD128(qa + 2048, qs), a2 = LD128(qa, qs + 4096), a3 = LD128(qa + 2048, qs + 4096);
          a0 ^= tag12; a1 ^= tag12; a2 ^= tag12; a3 ^= tag12;
          const unsigned bad = or4u(a3, or4u(a2, or4u(a1, or4u(a0, 0u)))) & 0xFFFu;
#pragma unroll
          for (int i = 0; i < 4; ++i) cst[i] = ((fu(a0[i]) + fu(a1[i])) + fu(a2[i])) + fu(a3[i]);
          DONE_IF(all_zero(bad));
        }
        const v4u zz = {0, 0, 0, 0};
        LDS(v4u, L_X + 896) = zz;
      }
      mup = 0.f;
      load_wq();
      BAR();
      xc = LDS(v4u, xoff);
    }
    FLUSH_STAMPS();
    __builtin_amdgcn_endpgm();
  }
  {
    const int wave = wv - 4, lane = lane_, c = lane & 15, G = lane >> 4;
    v4u K0, K1, K2, K3, K4, K5, K6, K7, V0, V1, V2, V3, V4, V5, V6, V7;
    {
      const unsigned kvo = (unsigned)(g * SK + (SK - WIN) + r * 128 + wave * 16) * 512u;
      const u64 kbp = PTR(ka, 1) + kvo, vb = PTR(ka, 2) + kvo;
      const unsigned ko = c * 512 + G * 16, vo = G * 2048 + c * 16;
      KVLD(K0, ko, kbp, 0); THR(1); KVLD(K1, ko, kbp, 64); THR(2); KVLD(K2, ko, kbp, 128); THR(3); KVLD(K3, ko, kbp, 192); THR(4);
      KVLD(K4, ko, kbp, 256); THR(5); KVLD(K5, ko, kbp, 320); THR(6); KVLD(K6, ko, kbp, 384); THR(7); KVLD(K7, ko, kbp, 448); THR(8);
      KVLD(V0, vo, vb, 0); THR(9); KVLD(V1, vo, vb, 256); THR(10); KVLD(V2, vo, vb, 512); THR(11); KVLD(V3, vo, vb, 768); THR(12);
      KVLD(V4, vo, vb, 1024); THR(13); KVLD(V5, vo, vb, 1280); THR(14); KVLD(V6, vo, vb, 1536); THR(15); KVLD(V7, vo, vb, 1792);
    }
    load_epoch();
    STAMP(1);
    unsigned pm[4];
#pragma unroll
    for (int q = 0; q < 4; ++q) pm[q] = (((c & 7) >> 1) == q) ? ((c & 1) ? 0xFFFF0000u : 0x0000FFFFu) : 0u;
    const int col = wave * 64 + c * 4 + G;

#pragma unroll 1
    for (int layer = 0; layer < 2; ++layer) {
      const int lo = layer * LAY, lx = layer * 8192;
      BAR();
      STAMP(32 + layer);
      {
        const int qo = L_Q + (c >> 3) * QSTR + G * 16;
        const v4u q0 = LDS(v4u, qo), q1 = LDS(v4u, qo + 64), q2 = LDS(v4u, qo + 128), q3 = LDS(v4u, qo + 192),
                  q4 = LDS(v4u, qo + 256), q5 = LDS(v4u, qo + 320), q6 = LDS(v4u, qo + 384), q7 = LDS(v4u, qo + 448);
        const float SC = LDS(float, L_SC);
        FENCE();
        asm volatile("s_waitcnt vmcnt(8)" : "+v"(K0), "+v"(K1), "+v"(K2), "+v"(K3), "+v"(K4), "+v"(K5), "+v"(K6), "+v"(K7));
        f32x4 s = {0, 0, 0, 0};
        s = mfma(K0, q0, s); s = mfma(K1, q1, s); s = mfma(K2, q2, s); s = mfma(K3, q3, s);
        s = mfma(K4, q4, s); s = mfma(K5, q5, s); s = mfma(K6, q6, s); s = mfma(K7, q7, s);
        float m = fmax2(fmax2(s[0], s[1]), fmax2(s[2], s[3]));
        m = swap16_max(m); m = swap32_max(m);
        m *= SC;
        float p0 = __builtin_amdgcn_exp2f(s[0] * SC - m), p1 = __builtin_amdgcn_exp2f(s[1] * SC - m),
              p2 = __builtin_amdgcn_exp2f(s[2] * SC - m), p3 = __builtin_amdgcn_exp2f(s[3] * SC - m);
        asm volatile("s_waitcnt vmcnt(0)" : "+v"(V0), "+v"(V1), "+v"(V2), "+v"(V3), "+v"(V4), "+v"(V5), "+v"(V6), "+v"(V7), "+v"(p0));
        f32x4 o0 = {0, 0, 0, 0}, o1 = {0, 0, 0, 0};
        unsigned pb; v4u pf;
        pb = pkbf(p0, p0); pf = (v4u){pb & pm[0], pb & pm[1], pb & pm[2], pb & pm[3]};
        o0 = mfma(V0, pf, o0); o1 = mfma(V1, pf, o1);
        pb = pkbf(p1, p1); pf = (v4u){pb & pm[0], pb & pm[1], pb & pm[2], pb & pm[3]};
        o0 = mfma(V2, pf, o0); o1 = mfma(V3, pf, o1);
        pb = pkbf(p2, p2); pf = (v4u){pb & pm[0], pb & pm[1], pb & pm[2], pb & pm[3]};
        o0 = mfma(V4, pf, o0); o1 = mfma(V5, pf, o1);
        pb = pkbf(p3, p3); pf = (v4u){pb & pm[0], pb & pm[1], pb & pm[2], pb & pm[3]};
        o0 = mfma(V6, pf, o0); o1 = mfma(V7, pf, o1);
        float l = (p0 + p1) + (p2 + p3);
        l = swap16_add(l, l); l = swap32_add(l, l);
        LDS(float, L_ML + (c >> 3) * 32 + wave * 4) = m;
        LDS(float, L_ML + 64 + (c >> 3) * 32 + wave * 4) = l;
        const int so = L_SLAB + wave * 2048 + (c >> 3) * 1024 + (G * 8 + (c & 7)) * 16;
        LDS(f32x4, so) = o0;
        LDS(f32x4, so + 512) = o1;
        STAMP(5 + layer * 16);
      }
      BAR();
      STAMP(34 + layer);
      {
        const int t4 = (wave * 64 + lane) * 4;
        const float mw = LDS(float, L_ML + (wave >> 2) * 32 + (c & 7) * 4);
        float ov[8];
#pragma unroll
        for (int w = 0; w < 8; ++w) ov[w] = LDS(float, L_SLAB + w * 2048 + t4);
        FENCE();
        float M = fmax2(mw, dpp<0xB1>(mw)); M = fmax2(M, dpp<0x4E>(M)); M = fmax2(M, dpp<0x141>(M));
        const float e = __builtin_amdgcn_exp2f(mw - M);
        float o = dpp<0x150>(e) * ov[0];
        o += dpp<0x151>(e) * ov[1]; o += dpp<0x152>(e) * ov[2]; o += dpp<0x153>(e) * ov[3];
        o += dpp<0x154>(e) * ov[4]; o += dpp<0x155>(e) * ov[5]; o += dpp<0x156>(e) * ov[6]; o += dpp<0x157>(e) * ov[7];
        ST32(TAGGED(o), t4, lo + WS_PO + (cu >> 2) * BLK + (cu & 3) * 2048 + (P0 ^ lx));
      }
      STAMP(6 + layer * 16);
      if (role == 1) {
        SLEEP(D2);
        const int a = lane >> 3, c8 = lane & 7;
        const int pb = (a >> 2) * BLK + (a & 3) * 2048 + wave * 128 + c8 * 16, ps = lo + WS_PO + go * 8 * BLK + hh * 1024 + (P0 ^ lx);
        v4u p0 = LD128(pb, ps), p1 = LD128(pb, ps + 2 * BLK), p2 = LD128(pb, ps + 4 * BLK), p3 = LD128(pb, ps + 6 * BLK);
        BAR();
        const f32x4 wt = LDS(f32x4, L_WT + a * 16);
        unsigned sp = 0;
        float o;
        POLL {
          p0 ^= tag12; p1 ^= tag12; p2 ^= tag12; p3 ^= tag12;
          float v[4];
#pragma unroll
          for (int i = 0; i < 4; ++i) v[i] = ((wt[0] * fu(p0[i]) + wt[1] * fu(p1[i])) + wt[2] * fu(p2[i])) + wt[3] * fu(p3[i]);
          const unsigned bad = or4u(p3, or4u(p2, or4u(p1, or4u(p0, 0u)))) & 0xFFFu;
          o = swap16_add(swap32_add(v[0], v[2]), swap32_add(v[1], v[3]));
          o += dpp<0x128>(o);
          DONE_IF(all_zero(bad));
          p0 = LD128(pb, ps); p1 = LD128(pb, ps + 2 * BLK); p2 = LD128(pb, ps + 4 * BLK); p3 = LD128(pb, ps + 6 * BLK);
        }
        LDS(u16, L_O + (wave * 32 + G * 8 + c8) * 2) = (u16)pkbf(o, o);
        CNT(41 + layer * 8, sp);
        STAMP(7 + layer * 16);
        BAR();
      } else if (role == 2) {
        if (layer == 0) BAR();
        BAR();
        if (wave < 7) {
          if (layer) SLEEP(D3);
          const int xb = G * 2048 + wave * 256 + c * 16, xs = lo + WS_X1P + (P1 ^ lx);
          const float xres = fu((unsigned)LDS(u16, L_X + col * 2) << 16);
          unsigned sp = 0;
          float x1;
          POLL {
            const v4u p0 = LD128(xb, xs), p1 = LD128(xb, xs + BLK), p2 = LD128(xb, xs + 2 * BLK), p3 = LD128(xb, xs + 3 * BLK);
            float v[4];
#pragma unroll
            for (int i = 0; i < 4; ++i) v[i] = ((fu(p0[i]) + fu(p1[i])) + fu(p2[i])) + fu(p3[i]);
            const unsigned mx = max4u(p3, max4u(p2, max4u(p1, max4u(p0, 0u))));
            x1 = xres + rbf(swap16_add(swap32_add(v[0], v[2]), swap32_add(v[1], v[3])));
            DONE_IF(none_eq(mx, SENT));
          }
          LDS(u16, L_X1 + col * 2) = (u16)pkbf(x1, x1);
          CNT(42 + layer * 8, sp);
          STAMP(9 + layer * 16);
          BAR();
          BAR();
        } else {
          int so = (c & 9) == 0 ? G * 16 + c * 2 : ((lane & 0x39) == 8 ? 64 + (c - 8) * 2 : WS_END);
          PIN(so);
          BAR();
          v4u x1c = LDS(v4u, L_X1 + lane * 16);
          const float cs = LDS(float, L_CS + lane * 4);
          const unsigned xw = LDS(unsigned, L_X1 + idg * 16 + (c & 6) * 2);
          if (lane >= 56) x1c = (v4u){0, 0, 0, 0};
          float mu, rstd;
          ln_stats(x1c, mu, rstd);
          float off = -mu * cs * rstd;
          PIN(off);
          BAR();
          const float raw = LDS(float, L_HM + lane * 4);
          const float gu = rbf(raw * rstd + off);
          const float sg = rbf(gu * __builtin_amdgcn_rcpf(1.f + __builtin_amdgcn_exp2f(-1.4426950408889634f * gu)));
          const float hv = sg * dpp<0x128>(gu);
          const unsigned hp = pkbf(hv, dpp<0xB1>(hv));
          ST32(NOSENT(c < 8 ? hp : xw), so, lo + WS_H + idg * 128 + (P1 ^ lx));
          STAMP(10 + layer * 16);
        }
      } else if (role == 4) {
        if (layer == 0) { BAR(); BAR(); }
      } else if (role == 3) {
        if (layer == 0) {
          BAR();
        } else {
          v4u W[7];
#pragma unroll
          for (int s = 0; s < 7; ++s) W[s] = trfrag(L_ROLE + wave * 7168 + s * 1024 + (8 * G + (c >> 2)) * 32 + (c & 3) * 8, 128);
          SLEEP(D4);
          const int hb = wave * 896 + G * 16;
          unsigned sp = 0;
          f32x4 acc;
          POLL {
            v4u hv[7];
            unsigned mx = 0;
#pragma unroll
            for (int s = 0; s < 7; ++s) hv[s] = LD128(hb + s * 128, LAY + WS_H + (P1 ^ 8192));
            FENCE();
            acc = (f32x4){0, 0, 0, 0};
#pragma unroll
            for (int s = 0; s < 7; ++s) { acc = mfma(hv[s], W[s], acc); mx = max4u(hv[s], mx); }
            DONE_IF(none_eq(mx, SENT));
          }
          LDS(float, L_DP + c * 32 + wave * 4) = acc[0];
          CNT(43 + layer * 8, sp);
          STAMP(11 + layer * 16);
          BAR();
        }
      }
      if (layer == 1) break;
      if (wave < 7) {
        check_flags();
        if (role == 0 || role == 3) SLEEP(D5);
        if (role == 1 || role == 4) SLEEP(D5 / 2);
        const int xb = G * 2048 + wave * 256 + c * 16;
        unsigned sp = 0;
        float x2;
        POLL {
          const v4u p = LD128(xb, WS_X2P + PG);
          x2 = swap16_add(swap32_add(fu(p[0]), fu(p[2])), swap32_add(fu(p[1]), fu(p[3])));
          DONE_IF(none_eq(max4u(p, 0u), SENT));
        }
        LDS(u16, L_X + col * 2) = (u16)pkbf(x2, x2);
        CNT(44, sp);
        STAMP(13);
      }
      BAR();
    }
    FLUSH_STAMPS();
  }
}

extern "C" void kernel_launch(void* const* d_in, const int* in_sizes, int n_in, void* d_out, int out_size, void* d_ws,
                              size_t ws_size, hipStream_t stream) {
  fused_decode<<<256, 768, 0, stream>>>((const u16*)d_in[0], (const u16*)d_in[1], (const u16*)d_in[2], (const u16*)d_in[3],
                                        (const u16*)d_in[4], (const u16*)d_in[6], (const u16*)d_in[7], (const u16*)d_in[8],
                                        (unsigned*)d_out, (unsigned char*)d_ws);
}
```

```cpp
#include <hip/hip_runtime.h>
#include <stdint.h>

typedef unsigned short u16;
typedef unsigned long long u64;
typedef unsigned v4u __attribute__((ext_vector_type(4)));
typedef unsigned v2u __attribute__((ext_vector_type(2)));
typedef unsigned v16u __attribute__((ext_vector_type(16)));
typedef short v4s __attribute__((ext_vector_type(4)));
typedef float f32x4 __attribute__((ext_vector_type(4)));
typedef float f32x2 __attribute__((ext_vector_type(2)));
typedef __bf16 bf16x8 __attribute__((ext_vector_type(8)));
typedef __bf16 bf16x2 __attribute__((ext_vector_type(2)));
#define LAS __attribute__((address_space(3)))
#define GAS __attribute__((address_space(1)))

constexpr int D = 448, SK = 65536, WIN = 4096, FF = 1792;

constexpr int LAY = 2 << 20, BLK = 16384;
constexpr int WS_EPOCH = 0;
constexpr int WS_X1 = 2048;
constexpr int WS_X2P = 1 * BLK;
constexpr int WS_H = 2 * BLK;
constexpr int WS_U = 107 * BLK;
constexpr int WS_X1P = 3 * BLK;
constexpr int WS_ST = 7 * BLK;
constexpr int WS_QP = 11 * BLK;
constexpr int WS_CS = 19 * BLK;
constexpr int WS_PO = 27 * BLK;
constexpr int WS_CAL = 91 * BLK;
constexpr int WS_END = 2 * LAY;
constexpr int WS_STAMP = 4 << 20;
constexpr int L_ML = 0, L_E = 512, L_IL = 576, L_SC = 704, L_PAR = 708, L_CAL = 720, L_FL = 752, L_CS = 768, L_HM = 1024, L_DP = 1280, L_O = 1792, L_Q = 2304, QSTR = 576,
              L_X = L_Q + 1152, L_X1 = L_X + 1024, L_BW = L_X1 + 1024, L_SLAB = L_BW + 2048, L_WQ = L_SLAB + 16384, L_ROLE = L_WQ + 28672, L_END = L_ROLE + 57344;

#define TDEF 14500
#define NWD2 30
#define N0GU 30
#define BIPOS 2
#define MAPK 0
#define DML 5
#define D6 8
#define DQ 4
#define D2 4
#define DST 4
#define D3 30
#define D4 0
#define D5 0
#define TH 0
#define TQ0 7500
#define PSL 1
#define CODE_BYTES 12400
constexpr unsigned SPIN_CAP = 1u << 17;
constexpr unsigned SENT = 0xFFFFFFFFu;

__device__ __forceinline__ f32x4 mfma(v4u a, v4u b, f32x4 c) {
  return __builtin_amdgcn_mfma_f32_16x16x32_bf16(__builtin_bit_cast(bf16x8, a), __builtin_bit_cast(bf16x8, b), c, 0, 0, 0);
}
__device__ __forceinline__ float dot2(unsigned a, unsigned b, float c) {
  return __builtin_amdgcn_fdot2_f32_bf16(__builtin_bit_cast(bf16x2, a), __builtin_bit_cast(bf16x2, b), c, false);
}
__device__ __forceinline__ unsigned pkbf(float lo, float hi) {
  f32x2 f = {lo, hi};
  return __builtin_bit_cast(unsigned, __builtin_convertvector(f, bf16x2));
}
__device__ __forceinline__ float fu(unsigned u) { return __builtin_bit_cast(float, u); }
__device__ __forceinline__ unsigned uf(float f) { return __builtin_bit_cast(unsigned, f); }
__device__ __forceinline__ float rbf(float f) { return fu(pkbf(f, f) & 0xFFFF0000u); }
template <int CTRL> __device__ __forceinline__ float dpp(float x) {
  return __builtin_bit_cast(float, __builtin_amdgcn_mov_dpp(__builtin_bit_cast(int, x), CTRL, 0xf, 0xf, true));
}
__device__ __forceinline__ float fmax2(float a, float b) { return __builtin_elementwise_maximum(a, b); }
__device__ __forceinline__ float swap32_add(float a, float b) {
  auto s = __builtin_amdgcn_permlane32_swap(uf(a), uf(b), false, false);
  return fu(s[0]) + fu(s[1]);
}
__device__ __forceinline__ float swap16_add(float a, float b) {
  auto s = __builtin_amdgcn_permlane16_swap(uf(a), uf(b), false, false);
  return fu(s[0]) + fu(s[1]);
}
__device__ __forceinline__ float swap32_max(float a) {
  auto s = __builtin_amdgcn_permlane32_swap(uf(a), uf(a), false, false);
  return fmax2(fu(s[0]), fu(s[1]));
}
__device__ __forceinline__ float swap16_max(float a) {
  auto s = __builtin_amdgcn_permlane16_swap(uf(a), uf(a), false, false);
  return fmax2(fu(s[0]), fu(s[1]));
}
__device__ __forceinline__ float row_sum16(float v) {
  v += dpp<0x128>(v); v += dpp<0x124>(v); v += dpp<0x122>(v); v += dpp<0x121>(v);
  return v;
}
__device__ __forceinline__ float row_max16(float v) {
  v = fmax2(v, dpp<0x128>(v)); v = fmax2(v, dpp<0x124>(v)); v = fmax2(v, dpp<0x122>(v)); v = fmax2(v, dpp<0x121>(v));
  return v;
}
__device__ __forceinline__ unsigned umax(unsigned a, unsigned b) { return a > b ? a : b; }
__device__ __forceinline__ unsigned umin(unsigned a, unsigned b) { return a < b ? a : b; }
__device__ __forceinline__ unsigned max4u(v4u v, unsigned acc) { return umax(umax(umax(umax(acc, v.x), v.y), v.z), v.w); }
__device__ __forceinline__ unsigned or4u(v4u v, unsigned acc) { return (acc | v.x | v.y) | v.z | v.w; }
__device__ __forceinline__ bool none_eq(unsigned v, unsigned x) { return __builtin_amdgcn_ballot_w64(v == x) == 0ull; }
__device__ __forceinline__ bool all_zero(unsigned v) { return __builtin_amdgcn_ballot_w64(v != 0u) == 0ull; }
__device__ __forceinline__ void glds16(unsigned voff, u64 sbase, unsigned lds_dst) {
  asm volatile("s_mov_b32 m0, %2\n\ts_nop 3\n\tglobal_load_lds_dwordx4 %0, %1 nt" :: "v"(voff), "s"(sbase), "s"(lds_dst) : "memory");
}
__device__ __forceinline__ void ln_stats(v4u x, float& mu, float& rstd) {
  const unsigned one2 = 0x3F803F80u;
  float s1 = dot2(x.x, one2, 0.f), s2 = dot2(x.x, x.x, 0.f);
  s1 = dot2(x.y, one2, s1); s2 = dot2(x.y, x.y, s2);
  s1 = dot2(x.z, one2, s1); s2 = dot2(x.z, x.z, s2);
  s1 = dot2(x.w, one2, s1); s2 = dot2(x.w, x.w, s2);
  float t = swap32_add(s1, s2);
  t = swap16_add(t, t);
  t = row_sum16(t);
  const float S1 = fu(__builtin_amdgcn_readlane(uf(t), 0)), S2 = fu(__builtin_amdgcn_readlane(uf(t), 32));
  const float mean = S1 * (1.f / D);
  const float var = fmax2(S2 * (1.f / D) - mean * mean, 0.f);
  mu = rbf(mean);
  const float den = rbf(__builtin_amdgcn_sqrtf(rbf(rbf(var) + 1e-5f)));
  rstd = __builtin_amdgcn_rcpf(den);
}
#define KVLD(dst, voff, sbase, imm) \
  asm volatile("global_load_dwordx4 %0, %1, %2 offset:" #imm " nt" : "=v"(dst) : "v"(voff), "s"(sbase) : "memory")
#define THR(n) do { if (TH) asm volatile("s_waitcnt vmcnt(%0)" :: "i"(TH > 0 ? TH - (n) : 0) : "memory"); } while (0)
#define BAR() do { asm volatile("s_waitcnt lgkmcnt(0)" ::: "memory"); __builtin_amdgcn_s_barrier(); asm volatile("" ::: "memory"); } while (0)
#define VMWAIT() asm volatile("s_waitcnt vmcnt(0)" ::: "memory")
#define FENCE() __builtin_amdgcn_sched_barrier(0)
#define PIN(x) asm volatile("" : "+v"(x))
#define LD128(off, so) __builtin_amdgcn_raw_buffer_load_b128(rs, (off), (so), 16)
#define LD32(off, so) __builtin_amdgcn_raw_buffer_load_b32(rs, (off), (so), 16)
#define ST128(v, off, so) __builtin_amdgcn_raw_buffer_store_b128((v), rs, (off), (so), 16)
#define ST64(v, off, so) __builtin_amdgcn_raw_buffer_store_b64((v), rs, (off), (so), 16)
#define ST32(v, off, so) __builtin_amdgcn_raw_buffer_store_b32((v), rs, (off), (so), 16)
#define ST32X2(v, off, so) do { const unsigned v_ = (v); ST32(v_, (off), (so)); ST32(v_, (off), (so) + 8192); } while (0)
#define SLEEP(n) do { if ((n) > 0) __builtin_amdgcn_s_sleep(n); } while (0)
#define POLL _Pragma("clang loop unroll(disable)") for (;;)
#define DONE_IF(cond) if (__builtin_expect((cond), 1) || ++sp > SPIN_CAP) break; __builtin_amdgcn_s_sleep(PSL)
#define LDS(T, off) (*(LAS T*)(L + (off)))
#define TAG16(u) (((u) & 0xFFFF0000u) | tag16)
#define TAGGED(f) ((uf(f) & 0xFFFFF000u) | tag12)

#define CHK(r0, r1, r2, r3, dst) \
  "v_bitop3_b32 v164, v" #r0 ", v" #r1 ", %[tag] bitop3:0x7e\n\t" \
  "v_bitop3_b32 v165, v" #r2 ", v" #r3 ", %[tag] bitop3:0x7e\n\t" \
  "v_bitop3_b32 v164, v164, v165, %[msk] bitop3:0xa8\n\t" \
  "v_cmp_ne_u32 " dst ", 0, v164\n\t"
__device__ __forceinline__ v4u poll1(v4u srd, int vo, int so, unsigned tag, unsigned msk, unsigned& sp) {
  v4u d;
  asm volatile(
      "s_mov_b32 %[sp], 0\n\ts_nop 3\n"
      ".Lp1_%=:\n\t"
      "buffer_load_dwordx4 v[96:99], %[vo], %[rs], %[so] offen sc1\n\t"
      "s_add_u32 %[sp], %[sp], 1\n\t"
      "s_cmp_gt_u32 %[sp], %c[cap]\n\t"
      "s_waitcnt vmcnt(0)\n\t"
      CHK(96, 97, 98, 99, "vcc")
      "s_cbranch_scc1 .Lp1d_%=\n\t"
      "s_cbranch_vccnz .Lp1_%=\n"
      ".Lp1d_%=:"
      : "={v[96:99]}"(d), [sp] "=&s"(sp)
      : [vo] "v"(vo), [rs] "s"(srd), [so] "s"(so), [tag] "s"(tag), [msk] "s"(msk), [cap] "i"(SPIN_CAP)
      : "memory", "scc", "vcc", "v164", "v165");
  return d;
}
__device__ __forceinline__ void poll2(v4u srd, int vo, int so0, int so1, unsigned tag, unsigned msk, v4u& d0, v4u& d1, unsigned& sp) {
  u64 m;
  asm volatile(
      "s_mov_b32 %[sp], 0\n\ts_nop 3\n"
      ".Lp2_%=:\n\t"
      "buffer_load_dwordx4 v[96:99], %[vo], %[rs], %[so0] offen sc1\n\t"
      "buffer_load_dwordx4 v[100:103], %[vo], %[rs], %[so1] offen sc1\n\t"
      "s_add_u32 %[sp], %[sp], 1\n\t"
      "s_waitcnt vmcnt(1)\n\t"
      CHK(96, 97, 98, 99, "%[m]")
      "s_waitcnt vmcnt(0)\n\t"
      CHK(100, 101, 102, 103, "vcc")
      "s_or_b64 vcc, vcc, %[m]\n\t"
      "s_cbranch_scc0 .Lp2d_%=\n\t"
      "s_cmp_gt_u32 %[sp], %c[cap]\n\t"
      "s_cbranch_scc0 .Lp2_%=\n"
      ".Lp2d_%=:"
      : "={v[96:99]}"(d0), "={v[100:103]}"(d1), [sp] "=&s"(sp), [m] "=&s"(m)
      : [vo] "v"(vo), [rs] "s"(srd), [so0] "s"(so0), [so1] "s"(so1), [tag] "s"(tag), [msk] "s"(msk), [cap] "i"(SPIN_CAP)
      : "memory", "scc", "vcc", "v164", "v165");
}
#define MF(a0, a3, b, cin) "v_mfma_f32_16x16x32_bf16 v[160:163], v[" #a0 ":" #a3 "], " b ", " cin "\n\t"
#define RELD(m, r0, r3, so) "s_mov_b64 exec, " m "\n\tbuffer_load_dwordx4 v[" #r0 ":" #r3 "], %[vo], %[rs], " so " offen sc1\n\t"
__device__ __forceinline__ f32x4 poll4_sum(v4u srd, int vo, int so, unsigned tag, v4u bone, f32x4 cin, unsigned& sp) {
  f32x4 acc; u64 m0, m1, m2, m3; int s1, s2, s3;
  asm volatile(
      "s_add_u32 %[s1], %[so], 0x4000\n\t"
      "s_add_u32 %[s2], %[so], 0x8000\n\t"
      "s_add_u32 %[s3], %[so], 0xc000\n\t"
      "s_mov_b32 %[sp], 0\n\t"
      "s_mov_b64 %[m0], -1\n\ts_mov_b64 %[m1], -1\n\ts_mov_b64 %[m2], -1\n\ts_mov_b64 %[m3], -1\n"
      ".Lp4_%=:\n\t"
      RELD("%[m0]", 96, 99, "%[so]") RELD("%[m1]", 100, 103, "%[s1]") RELD("%[m2]", 104, 107, "%[s2]") RELD("%[m3]", 108, 111, "%[s3]")
      "s_mov_b64 exec, -1\n\t"
      "s_add_u32 %[sp], %[sp], 1\n\t"
      "s_waitcnt vmcnt(3)\n\t" MF(96, 99, "%[b]", "%[c]") CHK(96, 97, 98, 99, "%[m0]")
      "s_waitcnt vmcnt(2)\n\t" MF(100, 103, "%[b]", "v[160:163]") CHK(100, 101, 102, 103, "%[m1]")
      "s_waitcnt vmcnt(1)\n\t" MF(104, 107, "%[b]", "v[160:163]") CHK(104, 105, 106, 107, "%[m2]")
      "s_or_b64 vcc, %[m0], %[m1]\n\t"
      "s_waitcnt vmcnt(0)\n\t" MF(108, 111, "%[b]", "v[160:163]") CHK(108, 109, 110, 111, "%[m3]")
      "s_or_b64 vcc, vcc, %[m2]\n\t"
      "s_or_b64 vcc, vcc, %[m3]\n\t"
      "s_cbranch_scc0 .Lp4d_%=\n\t"
      "s_cmp_gt_u32 %[sp], %c[cap]\n\t"
      "s_cbranch_scc0 .Lp4_%=\n"
      ".Lp4d_%=:\n\t"
      "s_nop 1"
      : "=&{v[160:163]}"(acc), [sp] "=&s"(sp), [m0] "=&s"(m0), [m1] "=&s"(m1), [m2] "=&s"(m2), [m3] "=&s"(m3), [s1] "=&s"(s1), [s2] "=&s"(s2), [s3] "=&s"(s3)
      : [vo] "v"(vo), [rs] "s"(srd), [so] "s"(so), [tag] "s"(tag), [msk] "s"(0xFFFFu), [b] "v"(bone), [c] "v"(cin), [cap] "i"(SPIN_CAP)
      : "memory", "scc", "vcc", "v164", "v165", "v96", "v97", "v98", "v99", "v100", "v101", "v102", "v103", "v104", "v105", "v106", "v107",
        "v108", "v109", "v110", "v111");
  return acc;
}
#define SOK(k) "s_add_u32 %[t], %[so], " #k "\n\t"
#define RELD8(m, r0, r3, k) SOK(k) RELD(m, r0, r3, "%[t]")
#define STEP8(n, r0, r1, r2, r3, b0, b3, cin, m) \
  "s_waitcnt vmcnt(" #n ") lgkmcnt(" #n ")\n\t" MF(r0, r3, "v[" #b0 ":" #b3 "]", cin) CHK(r0, r1, r2, r3, m)
__device__ __forceinline__ f32x4 poll8_wsum(v4u srd, int vo, int so, unsigned tag, int bo, unsigned& sp) {
  f32x4 acc; u64 m0, m1, m2, m3, m4, m5, m6, m7; int t;
  asm volatile(
      "s_mov_b32 %[sp], 0\n\t"
      "s_mov_b64 %[m0], -1\n\ts_mov_b64 %[m1], -1\n\ts_mov_b64 %[m2], -1\n\ts_mov_b64 %[m3], -1\n\t"
      "s_mov_b64 %[m4], -1\n\ts_mov_b64 %[m5], -1\n\ts_mov_b64 %[m6], -1\n\ts_mov_b64 %[m7], -1\n"
      ".Lp8_%=:\n\t"
      RELD("%[m0]", 96, 99, "%[so]") RELD8("%[m1]", 100, 103, 0x4000) RELD8("%[m2]", 104, 107, 0x8000) RELD8("%[m3]", 108, 111, 0xc000)
      RELD8("%[m4]", 112, 115, 0x10000) RELD8("%[m5]", 116, 119, 0x14000) RELD8("%[m6]", 120, 123, 0x18000) RELD8("%[m7]", 124, 127, 0x1c000)
      "s_mov_b64 exec, -1\n\t"
      "s_cmp_lg_u32 %[sp], 0\n\t"
      "s_cbranch_scc1 .Lp8c_%=\n\t"
      "s_barrier\n\t"
      "ds_read_b128 v[128:131], %[bo]\n\tds_read_b128 v[132:135], %[bo] offset:256\n\tds_read_b128 v[136:139], %[bo] offset:512\n\t"
      "ds_read_b128 v[140:143], %[bo] offset:768\n\tds_read_b128 v[144:147], %[bo] offset:1024\n\tds_read_b128 v[148:151], %[bo] offset:1280\n\t"
      "ds_read_b128 v[152:155], %[bo] offset:1536\n\tds_read_b128 v[156:159], %[bo] offset:1792\n"
      ".Lp8c_%=:\n\t"
      "s_add_u32 %[sp], %[sp], 1\n\t"
      STEP8(7, 96, 97, 98, 99, 128, 131, "0", "%[m0]") STEP8(6, 100, 101, 102, 103, 132, 135, "v[160:163]", "%[m1]")
      STEP8(5, 104, 105, 106, 107, 136, 139, "v[160:163]", "%[m2]") STEP8(4, 108, 109, 110, 111, 140, 143, "v[160:163]", "%[m3]")
      "s_or_b64 vcc, %[m0], %[m1]\n\t"
      STEP8(3, 112, 113, 114, 115, 144, 147, "v[160:163]", "%[m4]")
      "s_or_b64 vcc, vcc, %[m2]\n\t"
      STEP8(2, 116, 117, 118, 119, 148, 151, "v[160:163]", "%[m5]")
      "s_or_b64 vcc, vcc, %[m3]\n\t"
      "s_or_b64 vcc, vcc, %[m4]\n\t"
      STEP8(1, 120, 121, 122, 123, 152, 155, "v[160:163]", "%[m6]")
      "s_or_b64 vcc, vcc, %[m5]\n\t"
      STEP8(0, 124, 125, 126, 127, 156, 159, "v[160:163]", "%[m7]")
      "s_or_b64 vcc, vcc, %[m6]\n\t"
      "s_or_b64 vcc, vcc, %[m7]\n\t"
      "s_cbranch_scc0 .Lp8d_%=\n\t"
      "s_cmp_gt_u32 %[sp], %c[cap]\n\t"
      "s_cbranch_scc0 .Lp8_%=\n"
      ".Lp8d_%=:\n\t"
      "s_nop 1"
      : "=&{v[160:163]}"(acc), [sp] "=&s"(sp), [m0] "=&s"(m0), [m1] "=&s"(m1), [m2] "=&s"(m2), [m3] "=&s"(m3), [m4] "=&s"(m4), [m5] "=&s"(m5),
        [m6] "=&s"(m6), [m7] "=&s"(m7), [t] "=&s"(t)
      : [vo] "v"(vo), [rs] "s"(srd), [so] "s"(so), [tag] "s"(tag), [msk] "s"(0xFFFFu), [bo] "v"(bo), [cap] "i"(SPIN_CAP)
      : "memory", "scc", "vcc", "v164", "v165",
        "v96", "v97", "v98", "v99", "v100", "v101", "v102", "v103", "v104", "v105", "v106", "v107", "v108", "v109", "v110", "v111",
        "v112", "v113", "v114", "v115", "v116", "v117", "v118", "v119", "v120", "v121", "v122", "v123", "v124", "v125", "v126", "v127",
        "v128", "v129", "v130", "v131", "v132", "v133", "v134", "v135", "v136", "v137", "v138", "v139", "v140", "v141", "v142", "v143",
        "v144", "v145", "v146", "v147", "v148", "v149", "v150", "v151", "v152", "v153", "v154", "v155", "v156", "v157", "v158", "v159");
  return acc;
}

#define RSTAMP(k) do {} while (0)
#define STAMP(k) do {} while (0)
#define CNT(k, v) do {} while (0)
#define FLUSH_STAMPS() do {} while (0)

__global__ void __launch_bounds__(768)
fused_decode(const u16* xin_, const u16* kc_, const u16* vc_, const u16* wq_, const u16* wo_, const u16* wg_, const u16* wu_,
             const u16* wd_, unsigned* out_, unsigned char* ws_) {
  __shared__ __attribute__((aligned(16))) unsigned char smem[L_END];
  LAS unsigned char* L = (LAS unsigned char*)smem;
  const unsigned lbase = (unsigned)(uintptr_t)L;
  RSTAMP(60); STAMP(0);
  v16u ka; v4u kb4;
  int wv; unsigned so0, va, vb_;
  u64 pc0;
  {
    unsigned t0, t1, t2, v2, v3, v4, v5, v6;
    asm volatile(
        "s_load_dwordx16 %[ka], %[kp], 0x0\n\t"
        "s_load_dwordx4 %[kb], %[kp], 0x40\n\t"
        "v_readfirstlane_b32 %[wv], %[tid]\n\t"
        "s_getpc_b64 %[pc]\n\t"
        "v_lshlrev_b32 %[v2], 4, %[tid]\n\t"
        "s_and_b32 %[t2], %[wg], 7\n\t"
        "s_lshr_b32 %[wv], %[wv], 6\n\t"
        "s_cmp_lt_u32 %[wv], 4\n\t"
        "s_cbranch_scc1 .Lhead_svc%=\n\t"
        "s_lshl_b32 %[so], %[t2], 25\n\t"
        "s_lshr_b32 %[t0], %[wg], 3\n\t"
        "s_lshl_b32 %[t0], %[t0], %c[shr]\n\t"
        "s_lshl_b32 %[t1], %[wv], %c[shw]\n\t"
        "s_add_i32 %[so], %[so], %[t0]\n\t"
        "s_add_i32 %[so], %[so], %[t1]\n\t"
        "s_add_i32 %[so], %[so], %c[kvc]\n\t"
        "v_lshlrev_b32 %[va], 9, %[tid]\n\t"
        "v_and_b32 %[va], 0x1e00, %[va]\n\t"
        "v_and_or_b32 %[va], %[tid], 48, %[va]\n\t"
        "v_lshlrev_b32 %[vb], 7, %[tid]\n\t"
        "v_and_b32 %[vb], 0x1800, %[vb]\n\t"
        "v_and_b32 %[v2], 0xf0, %[v2]\n\t"
        "v_or_b32 %[vb], %[vb], %[v2]\n\t"
        "s_mov_b32 %[t2], 0\n\t"
        "s_branch .Lhead_end%=\n"
        ".Lhead_svc%=:\n\t"
        "s_setprio 3\n\t"
        "s_bfe_u32 %[t0], %[wg], 0x30003\n\t"
        "s_lshl_b32 %[t0], %[t0], 11\n\t"
        "v_lshl_add_u32 %[v3], %[tid], 3, %[t0]\n\t"
        "v_min_u32 %[v3], %c[clen], %[v3]\n\t"
        "s_movk_i32 m0, %c[lsl]\n\t"
        "v_and_b32 %[v3], -64, %[v3]\n\t"
        "global_load_lds_dword %[v3], %[pc]\n\t"
        "s_bfe_u32 %[t1], %[wg], 0x10006\n\t"
        "s_lshl_b32 %[t1], %[t1], 2\n\t"
        "s_add_i32 %[t1], %[t1], %[wv]\n\t"
        "s_mul_i32 %[so], %[t1], 0x70000\n\t"
        "s_cmpk_lt_u32 %[wg], 0x80\n\t"
        "s_cselect_b32 %[t0], 4, 1\n\t"
        "s_xor_b32 %[t0], %[t0], %[t2]\n\t"
        "s_lshl_b32 %[t0], %[t0], 10\n\t"
        "s_add_i32 %[so], %[so], %[t0]\n\t"
        "s_bfe_u32 %[t0], %[wg], 0x30003\n\t"
        "s_lshl_b32 %[t0], %[t0], 7\n\t"
        "s_add_i32 %[so], %[so], %[t0]\n\t"
        "v_lshlrev_b32 %[va], 10, %[tid]\n\t"
        "v_and_b32 %[va], 0xe000, %[va]\n\t"
        "v_and_b32 %[v2], 0x70, %[v2]\n\t"
        "v_or_b32 %[va], %[va], %[v2]\n\t"
        "v_add_u32 %[va], 0xc00, %[va]\n\t"
        "s_lshl_b32 %[t0], %[wv], 2\n\t"
        "v_mov_b32 %[v2], %[t0]\n\t"
        "v_mov_b32 %[v3], 0\n\t"
        "ds_write_b32 %[v2], %[v3] offset:%c[lfl]\n\t"
        "v_add_u32 %[vb], 0xfc00, %[va]\n\t"
        "v_add_u32 %[v4], 0x3f000, %[va]\n\t"
        "v_add_u32 %[v5], 0x4ec00, %[va]\n\t"
        "v_add_u32 %[v6], 0x5e800, %[va]\n\t"
        "s_mul_i32 %[t0], %[wv], 0x1c00\n\t"
        "s_add_i32 %[t0], %[t0], %c[lwq]\n\t"
        "s_mov_b32 m0, %[t0]\n\t"
        "s_waitcnt lgkmcnt(0)\n\t"
        "v_add_u32 %[v2], 0x1f800, %[va]\n\t"
        "v_add_u32 %[v3], 0x2f400, %[va]\n"
        ".Lhead_end%=:\n\t"
        "s_waitcnt lgkmcnt(0)"
        : [ka] "=&s"(ka), [kb] "=&s"(kb4), [wv] "=&s"(wv), [pc] "=&s"(pc0), [t0] "=&s"(t0), [t1] "=&s"(t1), [t2] "=&s"(t2),
          [so] "=&s"(so0), [va] "=&v"(va), [vb] "=&v"(vb_), [v2] "=&v"(v2), [v3] "=&v"(v3), [v4] "=&v"(v4), [v5] "=&v"(v5), [v6] "=&v"(v6)
        : [kp] "s"(__builtin_amdgcn_kernarg_segment_ptr()), [wg] "s"(blockIdx.x), [tid] "v"(threadIdx.x), [lfl] "i"(L_FL), [lwq] "i"(L_WQ + 3072), [lsl] "i"(L_SLAB), [clen] "i"(CODE_BYTES), [shr] "i"(MAPK ? 13 : 16), [shw] "i"(MAPK ? 18 : 13), [kvc] "i"(0x1e00000 - (MAPK ? (4 << 18) : (4 << 13)))
        : "memory", "scc");
    asm volatile("" : "+s"(ka), "+s"(kb4), "+s"(wv), "+s"(so0), "+s"(pc0), "+s"(t2));
#define PTR(v, i) ((u64)(v)[2 * (i)] | ((u64)(v)[2 * (i) + 1] << 32))
    asm volatile(
        "s_cmp_lt_u32 %[t2], 4\n\t"
        "s_cbranch_scc1 .Lhead_noq%=\n\t"
        "global_load_lds_dwordx4 %[va], %[src] offset:-3072 nt\n\t"
        "global_load_lds_dwordx4 %[vb], %[src] offset:-2048 nt\n\t"
        "global_load_lds_dwordx4 %[v2], %[src] offset:-1024 nt\n\t"
        "global_load_lds_dwordx4 %[v3], %[src] nt\n\t"
        "global_load_lds_dwordx4 %[v4], %[src] offset:1024 nt\n\t"
        "global_load_lds_dwordx4 %[v5], %[src] offset:2048 nt\n\t"
        "global_load_lds_dwordx4 %[v6], %[src] offset:3072 nt\n"
        ".Lhead_noq%=:\n\t"
        "s_cmp_eq_u32 %[wv], 3\n\t"
        "s_cbranch_scc0 .Lhead_nox%=\n\t"
        "s_movk_i32 m0, %c[lx]\n\t"
        "v_min_u32 %[v2], 0xf7, %[tid]\n\t"
        "v_lshlrev_b32 %[v2], 4, %[v2]\n\t"
        "v_and_b32 %[v2], 0x3f0, %[v2]\n\t"
        "global_load_lds_dwordx4 %[v2], %[xp]\n"
        ".Lhead_nox%=:\n\t"
        "s_barrier"
        : [v2] "+v"(v2)
        : [t2] "s"(t2), [wv] "s"(wv), [va] "v"(va), [vb] "v"(vb_), [v3] "v"(v3), [v4] "v"(v4), [v5] "v"(v5), [v6] "v"(v6),
          [src] "s"(PTR(ka, 3) + so0), [xp] "s"(PTR(ka, 0)), [tid] "v"(threadIdx.x), [lx] "i"(L_X)
        : "memory", "scc");
  }
  unsigned epoch, epoch_raw;
  asm volatile("s_load_dword %0, %1, 0x0" : "=s"(epoch_raw) : "s"(PTR(kb4, 1)) : "memory");
  const bool svc = wv < 4;
  v4u K0, K1, K2, K3, K4, K5, K6, K7, V0, V1, V2, V3, V4, V5, V6, V7;
  if (!svc) {
    const u64 kbp = PTR(ka, 1) + so0, vb = PTR(ka, 2) + so0;
    const unsigned ko = va, vo = vb_;
    KVLD(K0, ko, kbp, 0); KVLD(K1, ko, kbp, 64); KVLD(K2, ko, kbp, 128); KVLD(K3, ko, kbp, 192); THR(2);
    KVLD(K4, ko, kbp, 256); KVLD(K5, ko, kbp, 320); THR(2); KVLD(K6, ko, kbp, 384); KVLD(K7, ko, kbp, 448); THR(2);
    if (BIPOS == 0) __builtin_amdgcn_s_barrier();
    KVLD(V0, vo, vb, 0); KVLD(V1, vo, vb, 256);
    if (BIPOS == 2) __builtin_amdgcn_s_barrier();
    THR(2); KVLD(V2, vo, vb, 512); KVLD(V3, vo, vb, 768);
    if (BIPOS == 4) __builtin_amdgcn_s_barrier();
    KVLD(V4, vo, vb, 1024); KVLD(V5, vo, vb, 1280); KVLD(V6, vo, vb, 1536); KVLD(V7, vo, vb, 1792);
  }
  int b = blockIdx.x;
  asm volatile("" : "+s"(b));
  const int lane_ = threadIdx.x & 63;
  const int g = b & 7, r = b >> 3, cu = g * 32 + r;
  const int gq = r < 16 ? g ^ 4 : g ^ 1, rb = (r >> 3) & 1, cbq = r & 7, s0 = rb * 7;
  const u64 wsp = PTR(kb4, 1);
  const __amdgpu_buffer_rsrc_t rs = __builtin_amdgcn_make_buffer_rsrc((void*)wsp, 0, WS_END, 0x00020000);
  const v4u srd = {kb4[2], kb4[3] & 0xFFFFu, (unsigned)WS_END, 0x00020000u};
  const int role = (r < 28 || g == 6) ? (int)((0x34221111u >> (g * 4)) & 7) : 0;
  const bool real = r < 28;
  const int q7 = (r * 37) >> 8, cbo = r - 7 * q7;
  const int go = (q7 & 2) ? 4 + g : (g ^ 1), hh = q7 & 1, h = 2 * go + hh;
  const int idg = (g - 4) * 28 + r, kb = r & 3, cbd = real ? r >> 2 : 0;
  auto trfrag = [&](int addr, int step4) -> v4u {
    v4s lo = __builtin_amdgcn_ds_read_tr16_b64_v4i16((LAS v4s*)(L + addr));
    v4s hi = __builtin_amdgcn_ds_read_tr16_b64_v4i16((LAS v4s*)(L + addr + step4));
    v2u a = __builtin_bit_cast(v2u, lo), bb = __builtin_bit_cast(v2u, hi);
    return (v4u){a.x, a.y, bb.x, bb.y};
  };
  unsigned tag12, tag16;
  auto load_epoch = [&]() {
    asm volatile("s_waitcnt lgkmcnt(0)\n\ts_add_u32 %0, %1, 1" : "=s"(epoch) : "s"(epoch_raw) : "memory", "scc");
    tag12 = epoch & 0xFFFu; tag16 = epoch & 0xBFFFu;
  };
  int par = 0;

  if (svc) {
    const u64 tstart = __builtin_amdgcn_s_memtime();
    const unsigned one2 = 0x3F803F80u;
    const v4u ones = {one2, one2, one2, one2};
    const int calo = WS_CAL + (cu >> 4) * BLK + (cu & 15) * 512 + wv * 128;
    int xoff, wqoff, qsto, qldo, qwo;
    {
      const int lane = lane_, c = lane & 15, G = lane >> 4;
      ST128(ones, lane < 16 ? (lane >> 3) * 8192 + (lane & 7) * 16 : WS_END, calo);
      int ks = c + s0; ks -= ks >= 14 ? 14 : 0;
      xoff = c < 14 ? L_X + (4 * ks + G) * 16 : L_X + 896;
      wqoff = L_WQ + (8 * G + (c >> 2)) * 128 + wv * 32 + (c & 3) * 8;
      qsto = lane < 32 && g >= 4 ? gq * BLK + rb * 2048 + cbq * 256 + wv * 64 + c * 4 + G * 8192 : WS_END;
      qldo = g * BLK + (lane >> 5) * 2048 + wv * 512 + (lane & 31) * 16;
      qwo = L_Q + (wv >> 1) * QSTR + (wv & 1) * 256 + (lane & 31) * 8 + (lane >> 5) * 4;
    }
    load_epoch();
    asm volatile("s_waitcnt vmcnt(1)" ::: "memory");
    *(volatile LAS unsigned*)(L + L_FL + wv * 4) = 1u;
    {
      unsigned sp = 0;
      POLL {
        const v4u f = *(volatile LAS v4u*)(L + L_FL);
        if (__builtin_amdgcn_readfirstlane(f.x & f.y & f.z & f.w) || ++sp > (SPIN_CAP << 4)) break;
        __builtin_amdgcn_s_sleep(1);
      }
      asm volatile("" ::: "memory");
    }
    STAMP(1);
    v4u Wq[7], xc;
    float csq, mup;
    f32x4 cst = {0, 0, 0, 0};
    const v4u zz = {0, 0, 0, 0};
    auto load_wq = [&]() {
#pragma unroll
      for (int s = 0; s < 7; ++s) Wq[s] = trfrag(wqoff + s * 4096, 512);
    };
    load_wq();
    xc = LDS(v4u, xoff);
    if ((lane_ & 15) >= 14) xc = zz;
    {
      f32x4 a0 = {0, 0, 0, 0};
#pragma unroll
      for (int s = 0; s < 7; ++s) a0 = mfma(ones, Wq[s], a0);
      csq = a0[0];
      float rs_;
      ln_stats(xc, mup, rs_);
      mup *= csq;
    }
#pragma unroll 1
    for (int layer = 0; layer < 2; ++layer) {
      const int lo = layer * LAY, lx = layer * 8192;
      {
        const f32x4 z = {0, 0, 0, 0};
        const f32x4 d0 = mfma(xc, Wq[0], z), d4 = mfma(xc, Wq[4], z), d1 = mfma(xc, Wq[1], z), d5 = mfma(xc, Wq[5], z);
        const f32x4 d2 = mfma(xc, Wq[2], z), d6 = mfma(xc, Wq[6], z), d3 = mfma(xc, Wq[3], z);
        const float ta = (((d0[0] - mup) + d1[1]) + d2[2]) + d3[3], tb = (d4[0] + d5[1]) + d6[2];
        const float tq = (lane_ & 16) ? tb : ta;
        ST32(TAGGED(swap16_add(tq, tq)), qsto, lo + WS_QP);
        FENCE();
        STAMP(2 + layer * 16);
        float mu, rstd;
        ln_stats(xc, mu, rstd);
        LDS(float, L_SC) = rstd * (0.0625f * 1.4426950408889634f);
        if (wv == 0) { const v2u s2 = {SENT, SENT}; LDS(v2u, L_ML + lane_ * 8) = s2; }
        if (wv == 1 && layer == 0) { LDS(v4u, L_BW + lane_ * 16) = zz; LDS(v4u, L_BW + 1024 + lane_ * 16) = zz; }
        f32x4 init = cst * -mu;
        PIN(init);
        if (layer == 0) {
          asm volatile("s_waitcnt vmcnt(1)" ::: "memory");
          const u64 ca = wsp + calo + (wv & 1) * 8192, cb = wsp + calo + 8192 - (wv & 1) * 8192;
          u64 t0, t1, t2; unsigned j0, j1;
          asm volatile("s_memtime %0\n\ts_waitcnt lgkmcnt(0)\n\ts_load_dword %3, %5, 0x0\n\ts_waitcnt lgkmcnt(0)\n\ts_memtime %1\n\ts_waitcnt lgkmcnt(0)\n\t"
                       "s_load_dword %4, %6, 0x0\n\ts_waitcnt lgkmcnt(0)\n\ts_memtime %2\n\ts_waitcnt lgkmcnt(0)"
                       : "=&s"(t0), "=&s"(t1), "=&s"(t2), "=&s"(j0), "=&s"(j1) : "s"(ca), "s"(cb) : "memory");
          const unsigned da = (unsigned)(t1 - t0), db = (unsigned)(t2 - t1);
          LDS(unsigned, L_CAL + wv * 4) = (wv & 1) ? db : da;
          LDS(unsigned, L_CAL + 16 + wv * 4) = (wv & 1) ? da : db;
          CNT(56, (wv & 1) ? db : da); CNT(57, (wv & 1) ? da : db); CNT(58, 1 + (__builtin_amdgcn_s_getreg(20 | (3 << 11)) & 15));
          STAMP(37);
          if (BIPOS >= 0) __builtin_amdgcn_s_barrier();
          while (__builtin_amdgcn_s_memtime() - tstart < TQ0) __builtin_amdgcn_s_sleep(2);
          STAMP(38);
        } else {
          SLEEP(DQ);
        }
        const int qs = lo + WS_QP + (layer ? par ^ lx : 0);
        unsigned sp;
        const v4u a0 = poll1(srd, qldo, qs, tag12, 0xFFFu, sp) ^ tag12;
        LDS(unsigned, qwo) = pkbf(swap32_add(init[0] + fu(a0[0]), init[2] + fu(a0[2])), swap32_add(init[1] + fu(a0[1]), init[3] + fu(a0[3])));
        CNT(40 + layer * 8, sp);
        STAMP(3 + layer * 16);
      }
      BAR();
      int lane = lane_; PIN(lane);
      const int c = lane & 15, G = lane >> 4, trrow = 8 * G + (c >> 2), trp = c & 3;
      u64 sb = 0; unsigned vo = 0; int step = 0, n = 0, n0 = 0, pc = wv ? wv - 1 : 64;
      if (layer == 0) {
        const v4u cl = LDS(v4u, L_CAL), ch = LDS(v4u, L_CAL + 16);
        const unsigned ml = umin(umin(cl.x, cl.y), umin(cl.z, cl.w)), mh = umin(umin(ch.x, ch.y), umin(ch.z, ch.w));
        par = __builtin_amdgcn_readfirstlane(mh < ml ? 8192 : 0);
        CNT(59, 1 + (mh < ml));
        LDS(int, L_PAR) = par;
      }
      if (role == 3) { sb = PTR(ka, 7); vo = ((lane >> 1) * D + r * 16 + (lane & 1) * 8) * 2; step = 64 * D; n = layer ? 56 : NWD2; if (layer) pc += NWD2; }
      if (layer == 0) {
        if (role == 1) { sb = PTR(ka, 4); vo = ((h * 256 + (lane >> 3)) * D + cbo * 64 + (lane & 7) * 8) * 2; step = 16 * D; n = 32; n0 = 32; }
        else if (role == 2) { sb = (idg & 1) ? PTR(ka, 6) : PTR(ka, 5); vo = ((lane >> 3) * FF + (idg >> 1) * 64 + (lane & 7) * 8) * 2; step = 16 * FF; n = 56; n0 = N0GU; }
        else if (role == 4) { sb = PTR(ka, 7); vo = ((kb * 448 + (lane >> 3)) * D + cbd * 64 + (lane & 7) * 8) * 2; step = 16 * D; n = 56; n0 = 0; }
        if (role != 1 && __builtin_amdgcn_s_memtime() - tstart < TDEF) n0 = 0;
#pragma unroll 1
        for (; pc < n0; pc += 3) glds16(vo + pc * step, sb, lbase + L_ROLE + pc * 1024);
      }
      const int pl = par ^ lx;
      if (wv == 0) {
        int sto = (unsigned)(c - 4) < 8u && G < 2 ? (cu >> 6) * BLK + (cu & 63) * 128 + (c - 4) * 16 + G * 8192 : WS_END;
        PIN(sto);
        v4u ra, rb2;
        unsigned sp = 0;
        SLEEP(DML);
        POLL {
          ra = *(volatile LAS v4u*)(L + L_ML + c * 32); rb2 = *(volatile LAS v4u*)(L + L_ML + c * 32 + 16);
          if (none_eq(umax(umax(ra.x, ra.z), umax(rb2.x, rb2.z)), SENT) || ++sp > (SPIN_CAP << 4)) break;
        }
        const float mw = fu(ra.x);
        float M = fmax2(mw, dpp<0xB1>(mw)); M = fmax2(M, dpp<0x4E>(M)); M = fmax2(M, dpp<0x141>(M));
        M = __builtin_ceilf(M);
        const float e = __builtin_amdgcn_exp2f(mw - M);
        LDS(float, L_E + c * 4) = e;
        float ls = e * ((fu(ra.y) + fu(ra.w)) + (fu(rb2.y) + fu(rb2.w)));
        ls += dpp<0xB1>(ls); ls += dpp<0x4E>(ls); ls += dpp<0x141>(ls);
        const v4u rec = {(uf(M) & 0xFFFF0000u) | tag16, (uf(M) << 16) | tag16, (uf(ls) & 0xFFFF0000u) | tag16, (uf(ls) << 16) | tag16};
        ST128(rec, sto, lo + WS_ST);
        if (layer == 0) ST128(rec ^ 1u, sto, LAY + WS_ST);
        STAMP(17 + layer * 12);
        BAR();
      } else {
        BAR();
      }
      if (layer == 0 || role == 3) {
        BAR();
        if (layer == 0) {
          ST32(TAGGED(csq), qsto, WS_CS);
          ST32(tag12 ^ 1u, qsto, LAY + WS_QP);
          if (role == 1) {
            const int so = (lane >> 5) * LAY + (h >> 2) * BLK + (h & 3) * 2048 + cbo * 256 + wv * 64 + c * 4 + (G & 1) * 8192;
            ST32(tag16 ^ 1u, so, WS_X1P);
          } else if (role == 4 && real) {
            ST32(tag12 ^ 1u, lane < 32 ? G * 8192 + kb * 2048 + cbd * 256 + wv * 64 + c * 4 : WS_END, WS_X2P);
          }
        }
        if (wv) {
          while (__builtin_amdgcn_s_memtime() - tstart < TDEF) __builtin_amdgcn_s_sleep(2);
#pragma unroll 1
          for (; pc < n; pc += 3) glds16(vo + pc * step, sb, lbase + L_ROLE + pc * 1024);
        }
      }
      if (role == 1) {
        if (wv == 0) {
          SLEEP(DST);
          const int sa = (go >> 1) * BLK + ((go & 1) * 32 + (lane & 31)) * 128 + hh * 64;
          int bwo = L_BW + ((lane & 31) >> 2) * 256 + (lane & 3) * 64 + (lane >> 5) * 40;
          PIN(bwo);
          unsigned sp;
          const v4u st = poll1(srd, sa, lo + WS_ST + pl, tag16, 0xFFFFu, sp);
          CNT(46 + layer * 8, sp - 1);
          const float ms = fu(__builtin_amdgcn_perm(st.x, st.y, 0x07060302u));
          float M = row_max16(ms); M = swap16_max(M);
          const float e = fu(uf(__builtin_amdgcn_exp2f(ms - M)) & 0xFFFF0000u);
          LDS(float, bwo) = e; LDS(float, bwo + 20) = e;
          STAMP(4 + layer * 16);
          BAR();
          float Ls = row_sum16(e * fu(__builtin_amdgcn_perm(st.z, st.w, 0x07060302u))); Ls = swap16_add(Ls, Ls);
          LDS(float, L_IL) = __builtin_amdgcn_rcpf(Ls);
        } else {
          VMWAIT();
          BAR();
        }
        v4u W[8], A[8];
#pragma unroll
        for (int s = 0; s < 8; ++s) W[s] = trfrag(L_ROLE + s * 4096 + trrow * 128 + wv * 32 + trp * 8, 512);
        int so = lane < 32 ? (h >> 2) * BLK + (h & 3) * 2048 + cbo * 256 + wv * 64 + (c & 3) * 16 + (c >> 2) * 4 + G * 8192 : WS_END, ao = L_O + G * 16;
        PIN(so); PIN(ao);
        BAR();
#pragma unroll
        for (int s = 0; s < 8; ++s) A[s] = LDS(v4u, ao + s * 64);
        const float il = LDS(float, L_IL);
        FENCE();
        f32x4 acc = {0, 0, 0, 0};
#pragma unroll
        for (int s = 0; s < 8; ++s) acc = mfma(A[s], W[s], acc);
        { const float y = acc[0] * il; ST32(TAG16(pkbf(y, y)), so, lo + WS_X1P); }
        STAMP(8 + layer * 16);
      } else if (role == 2 || (role == 4 && layer == 0)) {
        VMWAIT();
        STAMP(role == 2 ? 14 + layer * 16 : 15);
        BAR();
        v4u W[14], A[14];
        int ho = wv * 64 + c * 4, ao = L_X1 + G * 16;
        int so = lane < 32 && real ? G * 8192 + kb * 2048 + cbd * 256 + wv * 64 + c * 4 : WS_END;
        {
          int wb = L_ROLE + trrow * 128 + wv * 32 + trp * 8;
          PIN(wb);
          f32x4 a = {0, 0, 0, 0};
#pragma unroll
          for (int s = 0; s < 14; ++s) { W[s] = trfrag(wb + s * 4096, 512); a = mfma(ones, W[s], a); }
          LDS(float, L_CS + ho) = a[0];
        }
        PIN(ho); PIN(ao); PIN(so);
        BAR();
#pragma unroll
        for (int s = 0; s < 14; ++s) A[s] = LDS(v4u, ao + s * 64);
        const float res = LDS(float, L_HM + ho);
        FENCE();
        f32x4 acc = {0, 0, 0, 0};
#pragma unroll
        for (int s = 0; s < 14; ++s) acc = mfma(A[s], W[s], acc);
        if (role == 2) {
          LDS(float, L_HM + ho) = acc[0];
        } else {
          ST32(TAGGED(acc[0] + res), so, WS_X2P);
          STAMP(12);
        }
        BAR();
      } else if (role == 3) {
        if (layer == 0) {
          VMWAIT();
          STAMP(16);
        } else {
          VMWAIT();
          STAMP(31);
          BAR();
          unsigned x1w = 0;
          if (wv == 0) {
            SLEEP(D4);
            unsigned sp = 0;
            POLL {
              x1w = LD32(r * 64 + c * 4, LAY + WS_X1 + pl) ^ tag16;
              DONE_IF(all_zero(x1w & 0xFFFFu));
            }
            PIN(x1w);
          }
          BAR();
          if (wv == 0) {
            const f32x4 pa = LDS(f32x4, L_DP + c * 32), pb = LDS(f32x4, L_DP + c * 32 + 16);
            const float y = ((pa[0] + pa[1]) + (pa[2] + pa[3])) + ((pb[0] + pb[1]) + (pb[2] + pb[3]));
            const float x2 = fu(x1w) + rbf(y);
            if (lane < 16) ((GAS u16*)PTR(kb4, 0))[r * 16 + lane] = (u16)pkbf(x2, x2);
            ST32(epoch, (r | lane) == 0 ? WS_EPOCH : WS_END, 0);
            STAMP(28);
          }
        }
      }
      if (layer == 1) break;
      {
        unsigned sp = 0;
        POLL {
          const v4u a0 = LD128(qldo, WS_CS) ^ tag12;
          cst = (f32x4){fu(a0[0]), fu(a0[1]), fu(a0[2]), fu(a0[3])};
          DONE_IF(all_zero(or4u(a0, 0u) & 0xFFFu));
        }
        LDS(v4u, L_X + 896) = zz;
      }
      mup = 0.f;
      load_wq();
      BAR();
      xc = LDS(v4u, xoff);
    }
    FLUSH_STAMPS();
    __builtin_amdgcn_endpgm();
  }
  {
    const int wave = wv - 4, lane = lane_, c = lane & 15, G = lane >> 4;
    if (wv < 8) __builtin_amdgcn_s_setprio(1);
    load_epoch();
    STAMP(1);
    unsigned pm[4];
#pragma unroll
    for (int q = 0; q < 4; ++q) pm[q] = (((c & 7) >> 1) == q) ? ((c & 1) ? 0xFFFF0000u : 0x0000FFFFu) : 0u;
    int slo;
    { const int d = (wave & 3) * 64 + G * 16 + (c & 3) * 4 + (c >> 2);
      slo = L_SLAB + (wave >> 2) * 1024 + (((d >> 5) * 8 + (d & 7)) * 4 + ((d >> 3) & 3)) * 4; }

#pragma unroll 1
    for (int layer = 0; layer < 2; ++layer) {
      const int lo = layer * LAY, lx = layer * 8192;
      BAR();
      STAMP(32 + layer);
      {
        const int qo = L_Q + (c >> 3) * QSTR + G * 16;
        const v4u q0 = LDS(v4u, qo), q1 = LDS(v4u, qo + 64), q2 = LDS(v4u, qo + 128), q3 = LDS(v4u, qo + 192),
                  q4 = LDS(v4u, qo + 256), q5 = LDS(v4u, qo + 320), q6 = LDS(v4u, qo + 384), q7 = LDS(v4u, qo + 448);
        const float SC = LDS(float, L_SC);
        FENCE();
        asm volatile("s_waitcnt vmcnt(8)" : "+v"(K0), "+v"(K1), "+v"(K2), "+v"(K3), "+v"(K4), "+v"(K5), "+v"(K6), "+v"(K7));
        f32x4 s = {0, 0, 0, 0};
        s = mfma(K0, q0, s); s = mfma(K1, q1, s); s = mfma(K2, q2, s); s = mfma(K3, q3, s);
        s = mfma(K4, q4, s); s = mfma(K5, q5, s); s = mfma(K6, q6, s); s = mfma(K7, q7, s);
        float m = fmax2(fmax2(fmax2(s[0], s[1]), s[2]), s[3]);
        m = swap16_max(m); m = swap32_max(m);
        m *= SC;
        float p0 = __builtin_amdgcn_exp2f(s[0] * SC - m), p1 = __builtin_amdgcn_exp2f(s[1] * SC - m),
              p2 = __builtin_amdgcn_exp2f(s[2] * SC - m), p3 = __builtin_amdgcn_exp2f(s[3] * SC - m);
        const f32x2 mlv = {m, (p0 + p1) + (p2 + p3)};
        LDS(f32x2, L_ML + (c >> 3) * 256 + wave * 32 + G * 8) = mlv;
        asm volatile("s_waitcnt vmcnt(0)" : "+v"(V0), "+v"(V1), "+v"(V2), "+v"(V3), "+v"(V4), "+v"(V5), "+v"(V6), "+v"(V7), "+v"(p0) :: "memory");
        f32x4 o0 = {0, 0, 0, 0}, o1 = {0, 0, 0, 0};
        unsigned pb; v4u pf;
        pb = pkbf(p0, p0); pf = (v4u){pb & pm[0], pb & pm[1], pb & pm[2], pb & pm[3]};
        o0 = mfma(V0, pf, o0); o1 = mfma(V1, pf, o1);
        pb = pkbf(p1, p1); pf = (v4u){pb & pm[0], pb & pm[1], pb & pm[2], pb & pm[3]};
        o0 = mfma(V2, pf, o0); o1 = mfma(V3, pf, o1);
        pb = pkbf(p2, p2); pf = (v4u){pb & pm[0], pb & pm[1], pb & pm[2], pb & pm[3]};
        o0 = mfma(V4, pf, o0); o1 = mfma(V5, pf, o1);
        pb = pkbf(p3, p3); pf = (v4u){pb & pm[0], pb & pm[1], pb & pm[2], pb & pm[3]};
        o0 = mfma(V6, pf, o0); o1 = mfma(V7, pf, o1);
        const int so = L_SLAB + wave * 2048 + (c >> 3) * 1024 + (G * 8 + (c & 7)) * 16;
        LDS(f32x4, so) = o0;
        LDS(f32x4, so + 512) = o1;
        STAMP(5 + layer * 16);
      }
      BAR();
      STAMP(34 + layer);
      {
        const int t4 = (wave * 64 + lane) * 4;
        const f32x4 ea = LDS(f32x4, L_E + (wave >> 2) * 32), eb = LDS(f32x4, L_E + (wave >> 2) * 32 + 16);
        float ov[8];
#pragma unroll
        for (int w = 0; w < 8; ++w) ov[w] = LDS(float, slo + w * 2048);
        const int pv = LDS(int, L_PAR);
        FENCE();
        float o = ea[0] * ov[0], o2 = ea[1] * ov[1];
        o += ea[2] * ov[2]; o2 += ea[3] * ov[3]; o += eb[0] * ov[4]; o2 += eb[1] * ov[5]; o += eb[2] * ov[6]; o2 += eb[3] * ov[7];
        o += o2;
        ST32X2(TAG16(pkbf(o, o)), t4, lo + WS_PO + (cu >> 2) * BLK + (cu & 3) * 2048);
        par = __builtin_amdgcn_readfirstlane(pv);
        if (layer == 0) ST32X2(tag16 ^ 1u, t4, LAY + WS_PO + (cu >> 2) * BLK + (cu & 3) * 2048);
      }
      STAMP(6 + layer * 16);
      const int pl = par ^ lx;
      if (layer == 0 || role == 3) BAR();
      {
      int lane = lane_; PIN(lane);
      const int c = lane & 15, G = lane >> 4;
      if (role == 1) {
        if (wave < 4) {
          SLEEP(D2);
          int ow = L_O + wave * 128 + G * 32 + (c & 3) * 8;
          PIN(ow);
          unsigned sp;
          const f32x4 o = poll8_wsum(srd, G * 2048 + wave * 256 + c * 16, lo + WS_PO + go * 8 * BLK + hh * 1024 + pl, tag16,
                                     L_BW + G * 64 + (c & 3) * 16, sp);
          const v2u ob = {pkbf(o[0], o[1]), pkbf(o[2], o[3])};
          LDS(v2u, ow) = ob;
          CNT(41 + layer * 8, sp - 1);
          STAMP(7 + layer * 16);
        } else {
          BAR();
        }
        BAR();
      } else if (role == 2) {
        BAR();
        if (wave < 7) {
          if (layer) SLEEP(D3);
          const int xo = wave * 128 + G * 32 + (c & 3) * 8;
          const v2u xr = LDS(v2u, L_X + xo);
          const f32x4 xres = {fu(xr.x << 16), fu(xr.x & 0xFFFF0000u), fu(xr.y << 16), fu(xr.y & 0xFFFF0000u)};
          const v4u bone = {(c & 3) == 0 ? 0x3F800000u : 0u, (c & 3) == 1 ? 0x3F800000u : 0u, (c & 3) == 2 ? 0x3F800000u : 0u, (c & 3) == 3 ? 0x3F800000u : 0u};
          unsigned sp;
          const f32x4 x1 = poll4_sum(srd, G * 2048 + wave * 256 + c * 16, lo + WS_X1P + pl, tag16, bone, xres, sp);
          const v2u xb = {pkbf(x1[0], x1[1]), pkbf(x1[2], x1[3])};
          LDS(v2u, L_X1 + xo) = xb;
          CNT(42 + layer * 8, sp);
          STAMP(9 + layer * 16);
          BAR();
          if (wave == 0 && idg < 14)
            ST32(((unsigned)LDS(u16, L_X1 + idg * 64 + (lane & 31) * 2) << 16) | tag16, (lane >> 5) * 8192 + (lane & 31) * 4, lo + WS_X1 + idg * 128);
          BAR();
        } else {
          if (layer == 0) {
            ST32X2(tag16 ^ 1u, lane * 4, ((idg & 1) ? WS_U : WS_H) + (idg >> 1) * 256);
            ST32X2(tag16 ^ 1u, lane * 4, LAY + ((idg & 1) ? WS_U : WS_H) + (idg >> 1) * 256);
          }
          BAR();
          v4u x1c = LDS(v4u, L_X1 + lane * 16);
          const float cs = LDS(float, L_CS + lane * 4);
          if (lane >= 56) x1c = (v4u){0, 0, 0, 0};
          float mu, rstd;
          ln_stats(x1c, mu, rstd);
          float off = -mu * cs * rstd;
          PIN(off);
          BAR();
          const float raw = LDS(float, L_HM + lane * 4);
          float gu = raw * rstd + off;
          if (!(idg & 1)) { gu = rbf(gu); gu = gu * __builtin_amdgcn_rcpf(1.f + __builtin_amdgcn_exp2f(-1.4426950408889634f * gu)); }
          ST32X2(TAG16(pkbf(gu, gu)), lane * 4, lo + ((idg & 1) ? WS_U : WS_H) + (idg >> 1) * 256);
          STAMP(10 + layer * 16);
        }
      } else if (role == 4) {
        if (layer == 0) {
          BAR();
          unsigned sp = 0;
          if (wave < 2) {
            const int ha = (kb * 14 + wave * 7) * 128 + umin(lane, 55) * 16;
            v4u hs, hu;
            poll2(srd, ha, WS_H + pl, WS_U + pl, tag16, 0xFFFFu, hs, hu, sp);
            hs ^= tag16; hu ^= tag16;
            const v2u hp = {pkbf(fu(hs.x) * fu(hu.x), fu(hs.y) * fu(hu.y)), pkbf(fu(hs.z) * fu(hu.z), fu(hs.w) * fu(hu.w))};
            LDS(v2u, L_X1 + wave * 448 + umin(lane, 55) * 8) = hp;
            CNT(43, sp);
            STAMP(11);
          } else if (wave == 2) {
            unsigned x1w = 0;
            if (kb == 0) {
              POLL {
                x1w = LD32(cbd * 256 + lane * 4, WS_X1 + pl) ^ tag16;
                DONE_IF(all_zero(x1w & 0xFFFFu));
              }
            }
            LDS(unsigned, L_HM + lane * 4) = x1w;
          }
          BAR();
          BAR();
        }
      } else if (role == 3) {
        if (layer == 1) {
          BAR();
          v4u W[7];
#pragma unroll
          for (int s = 0; s < 7; ++s) W[s] = trfrag(L_ROLE + wave * 7168 + s * 1024 + (8 * G + (c >> 2)) * 32 + (c & 3) * 8, 128);
          SLEEP(D4);
          const int hb = wave * 896 + umin(lane, 55) * 16;
          unsigned sp = 0;
          v4u hs, hu;
          poll2(srd, hb, LAY + WS_H + pl, LAY + WS_U + pl, tag16, 0xFFFFu, hs, hu, sp);
          hs ^= tag16; hu ^= tag16;
          const v2u hp = {pkbf(fu(hs.x) * fu(hu.x), fu(hs.y) * fu(hu.y)), pkbf(fu(hs.z) * fu(hu.z), fu(hs.w) * fu(hu.w))};
          LDS(v2u, L_WQ + wave * 512 + umin(lane, 55) * 8) = hp;
          v4u A[7];
#pragma unroll
          for (int s = 0; s < 7; ++s) A[s] = LDS(v4u, L_WQ + wave * 512 + s * 64 + G * 16);
          FENCE();
          f32x4 acc = {0, 0, 0, 0};
#pragma unroll
          for (int s = 0; s < 7; ++s) acc = mfma(A[s], W[s], acc);
          LDS(float, L_DP + c * 32 + wave * 4) = acc[0];
          CNT(43 + layer * 8, sp);
          STAMP(11 + layer * 16);
          BAR();
        }
      }
      if (layer == 1) break;
      if (wave < 7) {
        if (role == 4) SLEEP(D6);
        const int xb = G * 2048 + wave * 256 + c * 16;
        int xw = L_X + (wave * 64 + c * 4 + G) * 2;
        PIN(xw);
        unsigned sp;
        const v4u p = poll1(srd, xb, WS_X2P + par, tag12, 0xFFFu, sp) ^ tag12;
        const float x2 = swap16_add(swap32_add(fu(p[0]), fu(p[2])), swap32_add(fu(p[1]), fu(p[3])));
        LDS(u16, xw) = (u16)pkbf(x2, x2);
        CNT(44, sp);
        STAMP(13);
      }
      }
      BAR();
    }
    FLUSH_STAMPS();
  }
}

extern "C" void kernel_launch(void* const* d_in, const int* in_sizes, int n_in, void* d_out, int out_size, void* d_ws,
                              size_t ws_size, hipStream_t stream) {
  fused_decode<<<256, 768, 0, stream>>>((const u16*)d_in[0], (const u16*)d_in[1], (const u16*)d_in[2], (const u16*)d_in[3],
                                        (const u16*)d_in[4], (const u16*)d_in[6], (const u16*)d_in[7], (const u16*)d_in[8],
                                        (unsigned*)d_out, (unsigned char*)d_ws);
}
```
